```python
import torch

def _softmax(x, axis=-1):
    return torch.softmax(x, dim=axis)
def _gelu(x):
    return torch.nn.functional.gelu(x, approximate="tanh")
def _silu(x):
    return torch.nn.functional.silu(x)
def _sigmoid(x):
    return torch.sigmoid(x)
def _layernorm(x, axis=-1, eps=1e-5):
    m = x.mean(dim=axis, keepdim=True)
    v = x.var(dim=axis, keepdim=True, unbiased=False)
    return (x - m) / torch.sqrt(v + eps)
def _scatter_add(tgt, idx, src, axis=0):
    return tgt.clone().index_add_(axis, idx, src)
def _segment_sum(src, seg, n):
    out = torch.zeros((n,) + src.shape[1:], dtype=src.dtype)
    return out.index_add_(0, seg, src)
def _pad(x, pw):
    flat = tuple(v for pair in reversed(pw) for v in pair)
    return torch.nn.functional.pad(x, flat)

kernel_name = 'parallel_sum__gqa_block_cached__glu_gated_swiglu__decode'

def setup_inputs():

    _w = (lambda a: (lambda t: t.to(torch.bfloat16) if t.is_floating_point() else t)(torch.as_tensor(a)))
    def _A_setup():
        B, Sq, D = 4, 1, 512
        Hq, Hkv, Sk, Dh = 32, 8, 4096, 128
        assert Hq % Hkv == 0, (Hq, Hkv)
        return {
            "x": _w((lambda *s: torch.randn(*s, dtype=torch.bfloat16) * 0.05)(B, Sq, D)),
            "k_cache": _w((lambda *s: torch.randn(*s, dtype=torch.bfloat16) * 1.6)(B, Hkv, Sk, Dh)),
            "v_cache": _w((lambda *s: torch.randn(*s, dtype=torch.bfloat16) * 0.8)(B, Hkv, Sk, Dh)),
            "wq": _w((lambda *s: torch.randn(*s, dtype=torch.bfloat16) * 1.6)(D, Hq * Dh) * (D ** -0.5)),
            "wo": _w((lambda *s: torch.randn(*s, dtype=torch.bfloat16) * 0.8)(Hq * Dh, D) * ((Hq * Dh) ** -0.5)),
        }
    _A_in = _A_setup()

    def _B_setup():
        B, S, D, F = 4, 1, 512, 1792
        return {
            "x": _w((lambda *s: torch.randn(*s, dtype=torch.bfloat16) * 0.05)(B, S, D)),
            "w1": _w((lambda *s: torch.randn(*s, dtype=torch.bfloat16) * 0.4)(D, F) * (2.0 / D) ** 0.5),
            "w3": _w((lambda *s: torch.randn(*s, dtype=torch.bfloat16) * 0.4)(D, F) * (2.0 / D) ** 0.5),
            "w2": _w((lambda *s: torch.randn(*s, dtype=torch.bfloat16) * 0.4)(F, D) * (2.0 / F) ** 0.5),
        }
    _B_in = _B_setup()

    return {
        "A_x": _A_in["x"],
        "A_k_cache": _A_in["k_cache"],
        "A_v_cache": _A_in["v_cache"],
        "A_wq": _A_in["wq"],
        "A_wo": _A_in["wo"],
        "B_x": _B_in["x"],
        "B_w1": _B_in["w1"],
        "B_w3": _B_in["w3"],
        "B_w2": _B_in["w2"],
    }

def reference(A_x, A_k_cache, A_v_cache, A_wq, A_wo, B_x, B_w1, B_w3, B_w2):
    def _A_ref(x, k_cache, v_cache, wq, wo):
        B, Sq, D = x.shape
        Hq, Dh = 32, 128
        Hkv = k_cache.shape[1]
        rep = Hq // Hkv
        q = (lambda x, *d: x.permute(*d))((x @ wq).reshape(B, Sq, Hq, Dh), 0, 2, 1, 3)
        k = (lambda x, n, axis: torch.repeat_interleave(x, n, dim=axis))(k_cache, rep, axis=1)
        v = (lambda x, n, axis: torch.repeat_interleave(x, n, dim=axis))(v_cache, rep, axis=1)
        scores = torch.einsum("bhqd,bhkd->bhqk", q, k) * (Dh ** -0.5)
        p = _softmax(scores, axis=-1)
        o = torch.einsum("bhqk,bhkd->bhqd", p, v)
        return (lambda x, *d: x.permute(*d))(o, 0, 2, 1, 3).reshape(B, Sq, Hq * Dh) @ wo

    def _B_ref(x, w1, w3, w2):
        gate = torch.einsum("bsd,df->bsf", x, w1)
        up = torch.einsum("bsd,df->bsf", x, w3)
        h = _silu(gate) * up
        out = torch.einsum("bsf,fd->bsd", h, w2)
        return out

    x = A_x
    xn = _layernorm(x, axis=-1)
    return x + _A_ref(xn, A_k_cache, A_v_cache, A_wq, A_wo) + _B_ref(xn, B_w1, B_w3, B_w2)
```

```cpp
#include <hip/hip_runtime.h>
#include <hip/hip_bf16.h>
#include <stdint.h>

typedef unsigned short u16;
__device__ __forceinline__ float bf2f(u16 b) { return __builtin_bit_cast(float, (unsigned)b << 16); }
__device__ __forceinline__ u16 f2bf(float f) { unsigned u = __builtin_bit_cast(unsigned, f); u += 0x7FFFu + ((u >> 16) & 1u); return (u16)(u >> 16); }
__device__ __forceinline__ float rbf(float f) { return bf2f(f2bf(f)); }

constexpr int B = 4, D = 512, HQ = 32, HKV = 8, SK = 4096, DH = 128, F = 1792;

__global__ void k1(const u16* x, const u16* wq, const u16* w1, const u16* w3, float* q, float* h) {
  __shared__ float xn[B][D];
  __shared__ float red[4][B][64][2];
  int tid = threadIdx.x, lane = tid & 63, w = tid >> 6;
  {
    float v[8]; float s = 0;
    for (int j = 0; j < 8; ++j) { v[j] = bf2f(x[w * D + lane * 8 + j]); s += v[j]; }
    for (int o = 32; o; o >>= 1) s += __shfl_xor(s, o);
    float m = s / D; float ss = 0;
    for (int j = 0; j < 8; ++j) ss += (v[j] - m) * (v[j] - m);
    for (int o = 32; o; o >>= 1) ss += __shfl_xor(ss, o);
    float r = 1.0f / sqrtf(ss / D + 1e-5f);
    for (int j = 0; j < 8; ++j) xn[w][lane * 8 + j] = rbf((v[j] - m) * r);
  }
  __syncthreads();
  int blk = blockIdx.x;
  float a[B] = {0, 0, 0, 0}, c[B] = {0, 0, 0, 0};
  if (blk < 64) {
    int col = blk * 64 + lane;
    for (int k = w * 128; k < w * 128 + 128; ++k) { float wv = bf2f(wq[k * 4096 + col]); for (int b = 0; b < B; ++b) a[b] += xn[b][k] * wv; }
  } else {
    int col = (blk - 64) * 64 + lane;
    for (int k = w * 128; k < w * 128 + 128; ++k) { float wv = bf2f(w1[k * F + col]), uv = bf2f(w3[k * F + col]); for (int b = 0; b < B; ++b) { a[b] += xn[b][k] * wv; c[b] += xn[b][k] * uv; } }
  }
  for (int b = 0; b < B; ++b) { red[w][b][lane][0] = a[b]; red[w][b][lane][1] = c[b]; }
  __syncthreads();
  int b = w;
  float s0 = 0, s1 = 0;
  for (int i = 0; i < 4; ++i) { s0 += red[i][b][lane][0]; s1 += red[i][b][lane][1]; }
  if (blk < 64) q[b * 4096 + blk * 64 + lane] = rbf(s0);
  else { float g = rbf(s0), u = rbf(s1); h[b * F + (blk - 64) * 64 + lane] = rbf(rbf(g / (1.f + __expf(-g))) * u); }
}

__global__ void k2(const u16* kc, const u16* vc, const float* q, float* po, float* pml) {
  __shared__ float qs[4][128];
  __shared__ float p[4][128];
  __shared__ float red[4][2];
  int s = blockIdx.x, kvh = blockIdx.y, b = blockIdx.z, tid = threadIdx.x;
  for (int g = 0; g < 4; ++g) qs[g][tid] = q[b * 4096 + (kvh * 4 + g) * 128 + tid];
  __syncthreads();
  const u16* kr = kc + ((size_t)(b * HKV + kvh) * SK + s * 128 + tid) * DH;
  float sc[4] = {0, 0, 0, 0};
  for (int d = 0; d < 128; ++d) { float kv = bf2f(kr[d]); for (int g = 0; g < 4; ++g) sc[g] += kv * qs[g][d]; }
  float m[4], l[4];
  for (int g = 0; g < 4; ++g) {
    sc[g] *= 0.08838834764831845f;
    float mm = sc[g];
    for (int o = 32; o; o >>= 1) mm = fmaxf(mm, __shfl_xor(mm, o));
    if ((tid & 63) == 0) red[g][tid >> 6] = mm;
    __syncthreads();
    m[g] = fmaxf(red[g][0], red[g][1]);
    __syncthreads();
    float e = __expf(sc[g] - m[g]);
    p[g][tid] = e;
    for (int o = 32; o; o >>= 1) e += __shfl_xor(e, o);
    if ((tid & 63) == 0) red[g][tid >> 6] = e;
    __syncthreads();
    l[g] = red[g][0] + red[g][1];
    __syncthreads();
  }
  const u16* vr = vc + ((size_t)(b * HKV + kvh) * SK + s * 128) * DH + tid;
  float o[4] = {0, 0, 0, 0};
  for (int k = 0; k < 128; ++k) { float vv = bf2f(vr[k * DH]); for (int g = 0; g < 4; ++g) o[g] += p[g][k] * vv; }
  for (int g = 0; g < 4; ++g) {
    int row = (b * HQ + kvh * 4 + g) * 32 + s;
    po[row * 128 + tid] = o[g];
    if (tid == 0) { pml[row * 2] = m[g]; pml[row * 2 + 1] = l[g]; }
  }
}

__global__ void k3(const float* po, const float* pml, float* o) {
  int bh = blockIdx.x, tid = threadIdx.x;
  float M = -INFINITY;
  for (int s = 0; s < 32; ++s) M = fmaxf(M, pml[(bh * 32 + s) * 2]);
  float L = 0, acc = 0;
  for (int s = 0; s < 32; ++s) { float w = __expf(pml[(bh * 32 + s) * 2] - M); L += w * pml[(bh * 32 + s) * 2 + 1]; acc += w * po[(bh * 32 + s) * 128 + tid]; }
  o[bh * 128 + tid] = rbf(acc / L);
}

__global__ void k4(const u16* x, const float* o, const float* h, const u16* wo, const u16* w2, u16* out) {
  __shared__ float red[16][64][2];
  int cg = blockIdx.x, b = blockIdx.y, lane = threadIdx.x & 63, w = threadIdx.x >> 6;
  int col = cg * 64 + lane;
  float a = 0, c = 0;
  for (int k = w * 256; k < w * 256 + 256; ++k) a += o[b * 4096 + k] * bf2f(wo[k * D + col]);
  for (int k = w * 112; k < w * 112 + 112; ++k) c += h[b * F + k] * bf2f(w2[k * D + col]);
  red[w][lane][0] = a; red[w][lane][1] = c;
  __syncthreads();
  if (w == 0) {
    float s0 = 0, s1 = 0;
    for (int i = 0; i < 16; ++i) { s0 += red[i][lane][0]; s1 += red[i][lane][1]; }
    out[b * D + col] = f2bf(rbf(bf2f(x[b * D + col]) + rbf(s0)) + rbf(s1));
  }
}

extern "C" void kernel_launch(void* const* d_in, const int* in_sizes, int n_in, void* d_out, int out_size, void* d_ws, size_t ws_size, hipStream_t stream) {
  const u16* x = (const u16*)d_in[0];
  const u16* kc = (const u16*)d_in[1];
  const u16* vc = (const u16*)d_in[2];
  const u16* wq = (const u16*)d_in[3];
  const u16* wo = (const u16*)d_in[4];
  const u16* w1 = (const u16*)d_in[6];
  const u16* w3 = (const u16*)d_in[7];
  const u16* w2 = (const u16*)d_in[8];
  float* ws = (float*)d_ws;
  float* q = ws; float* h = q + 4 * 4096; float* o = h + 4 * F; float* pml = o + 4 * 4096; float* po = pml + 128 * 32 * 2;
  k1<<<64 + 28, 256, 0, stream>>>(x, wq, w1, w3, q, h);
  k2<<<dim3(32, 8, 4), 128, 0, stream>>>(kc, vc, q, po, pml);
  k3<<<128, 128, 0, stream>>>(po, pml, o);
  k4<<<dim3(8, 4), 1024, 0, stream>>>(x, o, h, wo, w2, (u16*)d_out);
}
```

```cpp
#include <hip/hip_runtime.h>
#include <hip/hip_bf16.h>
#include <stdint.h>

typedef unsigned short u16;
__device__ __forceinline__ float bf2f(u16 b) { return __builtin_bit_cast(float, (unsigned)b << 16); }
__device__ __forceinline__ u16 f2bf(float f) { unsigned u = __builtin_bit_cast(unsigned, f); u += 0x7FFFu + ((u >> 16) & 1u); return (u16)(u >> 16); }
__device__ __forceinline__ float rbf(float f) { return bf2f(f2bf(f)); }

constexpr int B = 4, D = 512, HQ = 32, HKV = 8, SK = 4096, DH = 128, F = 1792;

__global__ void k1(const u16* x, const u16* wq, const u16* w1, const u16* w3, float* q, float* h) {
  __shared__ float xn[B][D];
  __shared__ float red[4][B][64][2];
  int tid = threadIdx.x, lane = tid & 63, w = tid >> 6;
  {
    float v[8]; float s = 0;
    for (int j = 0; j < 8; ++j) { v[j] = bf2f(x[w * D + lane * 8 + j]); s += v[j]; }
    for (int o = 32; o; o >>= 1) s += __shfl_xor(s, o);
    float m = s / D; float ss = 0;
    for (int j = 0; j < 8; ++j) ss += (v[j] - m) * (v[j] - m);
    for (int o = 32; o; o >>= 1) ss += __shfl_xor(ss, o);
    float den = rbf(sqrtf(rbf(rbf(ss / D) + 1e-5f)));
    float mb = rbf(m);
    for (int j = 0; j < 8; ++j) xn[w][lane * 8 + j] = rbf(rbf(v[j] - mb) / den);
  }
  __syncthreads();
  int blk = blockIdx.x;
  float a[B] = {0, 0, 0, 0}, c[B] = {0, 0, 0, 0};
  if (blk < 64) {
    int col = blk * 64 + lane;
    for (int k = w * 128; k < w * 128 + 128; ++k) { float wv = bf2f(wq[k * 4096 + col]); for (int b = 0; b < B; ++b) a[b] += xn[b][k] * wv; }
  } else {
    int col = (blk - 64) * 64 + lane;
    for (int k = w * 128; k < w * 128 + 128; ++k) { float wv = bf2f(w1[k * F + col]), uv = bf2f(w3[k * F + col]); for (int b = 0; b < B; ++b) { a[b] += xn[b][k] * wv; c[b] += xn[b][k] * uv; } }
  }
  for (int b = 0; b < B; ++b) { red[w][b][lane][0] = a[b]; red[w][b][lane][1] = c[b]; }
  __syncthreads();
  int b = w;
  float s0 = 0, s1 = 0;
  for (int i = 0; i < 4; ++i) { s0 += red[i][b][lane][0]; s1 += red[i][b][lane][1]; }
  if (blk < 64) q[b * 4096 + blk * 64 + lane] = rbf(s0);
  else { float g = rbf(s0), u = rbf(s1); h[b * F + (blk - 64) * 64 + lane] = rbf(rbf(g / (1.f + __expf(-g))) * u); }
}

__global__ void k2(const u16* kc, const u16* vc, const float* q, float* po, float* pml) {
  __shared__ float qs[4][128];
  __shared__ float p[4][128];
  __shared__ float red[4][2];
  int s = blockIdx.x, kvh = blockIdx.y, b = blockIdx.z, tid = threadIdx.x;
  for (int g = 0; g < 4; ++g) qs[g][tid] = q[b * 4096 + (kvh * 4 + g) * 128 + tid];
  __syncthreads();
  const u16* kr = kc + ((size_t)(b * HKV + kvh) * SK + s * 128 + tid) * DH;
  float sc[4] = {0, 0, 0, 0};
  for (int d = 0; d < 128; ++d) { float kv = bf2f(kr[d]); for (int g = 0; g < 4; ++g) sc[g] += kv * qs[g][d]; }
  float m[4], l[4];
  for (int g = 0; g < 4; ++g) {
    sc[g] = rbf(rbf(sc[g]) * 0.08838834764831845f);
    float mm = sc[g];
    for (int o = 32; o; o >>= 1) mm = fmaxf(mm, __shfl_xor(mm, o));
    if ((tid & 63) == 0) red[g][tid >> 6] = mm;
    __syncthreads();
    m[g] = fmaxf(red[g][0], red[g][1]);
    __syncthreads();
    float e = __expf(sc[g] - m[g]);
    p[g][tid] = e;
    for (int o = 32; o; o >>= 1) e += __shfl_xor(e, o);
    if ((tid & 63) == 0) red[g][tid >> 6] = e;
    __syncthreads();
    l[g] = red[g][0] + red[g][1];
    __syncthreads();
  }
  const u16* vr = vc + ((size_t)(b * HKV + kvh) * SK + s * 128) * DH + tid;
  float o[4] = {0, 0, 0, 0};
  for (int k = 0; k < 128; ++k) { float vv = bf2f(vr[k * DH]); for (int g = 0; g < 4; ++g) o[g] += p[g][k] * vv; }
  for (int g = 0; g < 4; ++g) {
    int row = (b * HQ + kvh * 4 + g) * 32 + s;
    po[row * 128 + tid] = o[g];
    if (tid == 0) { pml[row * 2] = m[g]; pml[row * 2 + 1] = l[g]; }
  }
}

__global__ void k3(const float* po, const float* pml, float* o) {
  int bh = blockIdx.x, tid = threadIdx.x;
  float M = -INFINITY;
  for (int s = 0; s < 32; ++s) M = fmaxf(M, pml[(bh * 32 + s) * 2]);
  float L = 0, acc = 0;
  for (int s = 0; s < 32; ++s) { float w = __expf(pml[(bh * 32 + s) * 2] - M); L += w * pml[(bh * 32 + s) * 2 + 1]; acc += w * po[(bh * 32 + s) * 128 + tid]; }
  o[bh * 128 + tid] = rbf(acc / L);
}

__global__ void k4(const u16* x, const float* o, const float* h, const u16* wo, const u16* w2, u16* out) {
  __shared__ float red[16][64][2];
  int cg = blockIdx.x, b = blockIdx.y, lane = threadIdx.x & 63, w = threadIdx.x >> 6;
  int col = cg * 64 + lane;
  float a = 0, c = 0;
  for (int k = w * 256; k < w * 256 + 256; ++k) a += o[b * 4096 + k] * bf2f(wo[k * D + col]);
  for (int k = w * 112; k < w * 112 + 112; ++k) c += h[b * F + k] * bf2f(w2[k * D + col]);
  red[w][lane][0] = a; red[w][lane][1] = c;
  __syncthreads();
  if (w == 0) {
    float s0 = 0, s1 = 0;
    for (int i = 0; i < 16; ++i) { s0 += red[i][lane][0]; s1 += red[i][lane][1]; }
    out[b * D + col] = f2bf(rbf(bf2f(x[b * D + col]) + rbf(s0)) + rbf(s1));
  }
}

extern "C" void kernel_launch(void* const* d_in, const int* in_sizes, int n_in, void* d_out, int out_size, void* d_ws, size_t ws_size, hipStream_t stream) {
  const u16* x = (const u16*)d_in[0];
  const u16* kc = (const u16*)d_in[1];
  const u16* vc = (const u16*)d_in[2];
  const u16* wq = (const u16*)d_in[3];
  const u16* wo = (const u16*)d_in[4];
  const u16* w1 = (const u16*)d_in[6];
  const u16* w3 = (const u16*)d_in[7];
  const u16* w2 = (const u16*)d_in[8];
  float* ws = (float*)d_ws;
  float* q = ws; float* h = q + 4 * 4096; float* o = h + 4 * F; float* pml = o + 4 * 4096; float* po = pml + 128 * 32 * 2;
  k1<<<64 + 28, 256, 0, stream>>>(x, wq, w1, w3, q, h);
  k2<<<dim3(32, 8, 4), 128, 0, stream>>>(kc, vc, q, po, pml);
  k3<<<128, 128, 0, stream>>>(po, pml, o);
  k4<<<dim3(8, 4), 1024, 0, stream>>>(x, o, h, wo, w2, (u16*)d_out);
}
```

```cpp
#include <hip/hip_runtime.h>
#include <stdint.h>

typedef unsigned short u16;
typedef unsigned v4u __attribute__((ext_vector_type(4)));
typedef unsigned v2u __attribute__((ext_vector_type(2)));
typedef __bf16 bf16x8 __attribute__((ext_vector_type(8)));
typedef __bf16 bf16x2 __attribute__((ext_vector_type(2)));
typedef float f32x4 __attribute__((ext_vector_type(4)));
typedef float f32x2 __attribute__((ext_vector_type(2)));
#define LAS __attribute__((address_space(3)))

#define STAMP 0
#define KDEPTH 8
#define KDELAY 0
#define VD1 1
#define VD2 10
#define VSW 6
#define WOPOS 28
#define CODE_BYTES 12288
#define PFMASK 3
#define POLL_SLEEP 8
#define PDELAY 2

namespace {
constexpr int AUX_NT = 2, AUX_SC1 = 16, RSRC_W3 = 0x00020000;
constexpr int NTHR = 512;
constexpr unsigned SPIN_CAP = 1u << 16;
constexpr int WS_E = 0;
constexpr int WS_PG = 4096;
constexpr int WS_ML = WS_PG + 240 * 2048;
constexpr int WS_OG = WS_ML + 256 * 128;
constexpr int WS_YG = WS_OG + 128 * 8 * 1024;
constexpr int WS_ST = WS_YG + 4 * 64 * 4096;
constexpr int WS_BYTES = WS_ST + 256 * 8 * 8 * 8;
constexpr int L_ORED = 0;
constexpr int L_PX = L_ORED + 8192;
constexpr int L_P = L_PX + 3072;
constexpr int L_QS = L_P + 4096;
constexpr int L_MX = L_QS + 1024;
constexpr int L_LS = L_MX + 64;
constexpr int L_OA = L_LS + 64;
constexpr int L_HA = L_OA + 512;
constexpr int L_FLAG = L_HA + 256;
constexpr int L_BYTES = L_FLAG + 32;

constexpr float LOG2E = 1.4426950408889634f;
constexpr float SCALE = 0.08838834764831845f;

__device__ __forceinline__ float asf(unsigned u) { return __builtin_bit_cast(float, u); }
__device__ __forceinline__ unsigned asu(float f) { return __builtin_bit_cast(unsigned, f); }
__device__ __forceinline__ unsigned pk2(float lo, float hi) {
  f32x2 p = {lo, hi};
  return __builtin_bit_cast(unsigned, __builtin_convertvector(p, bf16x2));
}
__device__ __forceinline__ float lo16(unsigned u) { return asf(u << 16); }
__device__ __forceinline__ float hi16(unsigned u) { return asf(u & 0xffff0000u); }
__device__ __forceinline__ float rbf(float f) { return lo16(pk2(f, f)); }
template <int CTRL> __device__ __forceinline__ unsigned dppu(unsigned x) { return (unsigned)__builtin_amdgcn_mov_dpp((int)x, CTRL, 0xf, 0xf, true); }
template <int CTRL> __device__ __forceinline__ float dpp(float x) { return asf(dppu<CTRL>(asu(x))); }
constexpr int XOR1 = 0xB1, XOR2 = 0x4E, ROR8 = 0x128, ROR4 = 0x124;
__device__ __forceinline__ float opq(float x) { asm("" : "+v"(x)); return x; }
__device__ __forceinline__ float half_sum(float x) {
  auto t = __builtin_amdgcn_permlane32_swap(asu(x), asu(x), false, false);
  return asf(t[0]) + asf(t[1]);
}
__device__ __forceinline__ float xrow_sum(float x) {
  auto s = __builtin_amdgcn_permlane16_swap(asu(x), asu(x), false, false);
  return half_sum(asf(s[0]) + asf(s[1]));
}
__device__ __forceinline__ float xrow_max(float x) {
  auto s = __builtin_amdgcn_permlane16_swap(asu(x), asu(x), false, false);
  x = fmaxf(asf(s[0]), asf(s[1]));
  auto t = __builtin_amdgcn_permlane32_swap(asu(x), asu(x), false, false);
  return fmaxf(asf(t[0]), asf(t[1]));
}
__device__ __forceinline__ f32x4 mfma(v4u a, v4u b, f32x4 c) {
  return __builtin_amdgcn_mfma_f32_16x16x32_bf16(__builtin_bit_cast(bf16x8, a), __builtin_bit_cast(bf16x8, b), c, 0, 0, 0);
}
__device__ __forceinline__ unsigned permlo(unsigned even, unsigned odd) { return __builtin_amdgcn_perm(odd, even, 0x05040100u); }
__device__ __forceinline__ unsigned permhi(unsigned even, unsigned odd) { return __builtin_amdgcn_perm(odd, even, 0x07060302u); }
__device__ __forceinline__ unsigned permsel(const v4u& e, const v4u& o, int i) { return (i & 1) ? permhi(e[i >> 1], o[i >> 1]) : permlo(e[i >> 1], o[i >> 1]); }
#define PIN() __builtin_amdgcn_sched_barrier(0)
#define LGKM0() asm volatile("s_waitcnt lgkmcnt(0)" ::: "memory")
#define VMCNT(n) asm volatile("s_waitcnt vmcnt(%0)" ::"i"(n) : "memory")
__device__ __forceinline__ void wg_barrier() {
  PIN();
  LGKM0();
  __builtin_amdgcn_s_barrier();
  asm volatile("" ::: "memory");
  PIN();
}
typedef __amdgpu_buffer_rsrc_t rsrc_t;
__device__ __forceinline__ rsrc_t mkrsrc(const void* p, unsigned bytes) { return __builtin_amdgcn_make_buffer_rsrc((void*)p, 0, (int)bytes, RSRC_W3); }
__device__ __forceinline__ unsigned load_tag(const unsigned char* ws) {
  unsigned e;
  asm volatile("s_load_dword %0, %1, 0x0\n\ts_waitcnt lgkmcnt(0)" : "=s"(e) : "s"(ws) : "memory");
  return e + 1u;
}
__device__ __forceinline__ v4u gran2(float a, float b, unsigned tag, unsigned t8) {
  return v4u{(asu(a) & ~0xffu) | t8, tag, (asu(b) & ~0xffu) | t8, tag};
}
__device__ __forceinline__ void st16(rsrc_t rs, int off, v4u v) { __builtin_amdgcn_raw_buffer_store_b128(v, rs, off, 0, AUX_SC1); }
__device__ __forceinline__ bool gran_bad(const v4u& v, unsigned tag, unsigned t8) {
  return (((v[1] ^ tag) | (v[3] ^ tag)) | (((v[0] ^ t8) | (v[2] ^ t8)) & 0xffu)) != 0u;
}
template <int N, int SLEEP>
__device__ __forceinline__ void gather(rsrc_t rs, int soff, const int (&voff)[N], v4u (&v)[N], unsigned tag, unsigned t8) {
  bool bad[N];
#pragma unroll
  for (int k = 0; k < N; ++k) v[k] = __builtin_amdgcn_raw_buffer_load_b128(rs, voff[k], soff, AUX_SC1);
  for (unsigned sp = 0; sp < SPIN_CAP; ++sp) {
    bool any = false;
#pragma unroll
    for (int k = 0; k < N; ++k) { bad[k] = gran_bad(v[k], tag, t8); any |= bad[k]; }
    if (__builtin_amdgcn_ballot_w64(any) == 0ull) break;
    __builtin_amdgcn_s_sleep(SLEEP);
    asm volatile("" ::: "memory");
#pragma unroll
    for (int k = 0; k < N; ++k)
      if (bad[k]) v[k] = __builtin_amdgcn_raw_buffer_load_b128(rs, voff[k], soff, AUX_SC1);
  }
}
template <int SLEEP>
__device__ __forceinline__ void lds_wait(volatile LAS unsigned* f, unsigned want) {
#pragma clang loop unroll(disable)
  for (unsigned sp = 0; sp < (1u << 24); ++sp) {
    if ((unsigned)__builtin_amdgcn_readfirstlane(*f) >= want) break;
    __builtin_amdgcn_s_sleep(SLEEP);
  }
  asm volatile("" ::: "memory");
}
__device__ __forceinline__ void lds_inc(volatile LAS unsigned* f, int lane) {
  LGKM0();
  if (lane == 0) __hip_atomic_fetch_add((LAS unsigned*)f, 1u, __ATOMIC_RELAXED, __HIP_MEMORY_SCOPE_WORKGROUP);
}
__device__ __forceinline__ f32x4 fold8(const f32x4 (&acc)[8], bool rs) {
  f32x4 r;
#pragma unroll
  for (int i = 0; i < 4; ++i) {
    const float a0 = opq(acc[i][0]), a1 = opq(acc[i][1]), b0 = opq(acc[4 + i][0]), b1 = opq(acc[4 + i][1]);
    r[i] = (rs ? b1 : a0) + dpp<ROR8>(rs ? a1 : b0);
  }
  return r;
}
#define ST(i) do {} while (0)
#define STDUMP() do {} while (0)
}

__global__ void __launch_bounds__(NTHR, 2)
fused_decode(const u16* __restrict__ x, const u16* __restrict__ kc, const u16* __restrict__ vc, const u16* __restrict__ wq,
             const u16* __restrict__ wo, const u16* __restrict__ w1, const u16* __restrict__ w3, const u16* __restrict__ w2,
             u16* __restrict__ out, unsigned char* ws) {
  __shared__ __attribute__((aligned(16))) unsigned char smem[L_BYTES];
  LAS unsigned char* L = (LAS unsigned char*)smem;
  const int tid = threadIdx.x, lane = tid & 63;
  const int wave = __builtin_amdgcn_readfirstlane(tid >> 6), w4 = wave & 3;
  const int bid = blockIdx.x, xcd = bid & 7, idx = bid >> 3;
  const int g = lane >> 4, c = lane & 15;
  ST(0);
  unsigned pf = 0u;
  if (CODE_BYTES > 0 && wave >= 6 && (idx & PFMASK) == 0) {
    unsigned long long pc;
    asm volatile("s_getpc_b64 %0" : "=s"(pc));
    pf = __builtin_amdgcn_raw_buffer_load_b32(mkrsrc((const void*)(pc & ~255ull), CODE_BYTES), ((wave - 6) * 64 + lane) * 128, 0, 0);
  }
  const rsrc_t rws = mkrsrc(ws, WS_BYTES);
  volatile LAS unsigned* flag = (volatile LAS unsigned*)(L + L_FLAG);
  if (tid < 8) flag[tid] = 0u;
  asm volatile("" ::"s"(x), "s"(kc), "s"(vc), "s"(wq), "s"(wo), "s"(w1), "s"(w3), "s"(w2), "s"(out), "s"(ws));
  wg_barrier();
  const int b = idx >> 3, split = idx & 7;
  const int sbase = ((b * 8 + xcd) * 1024 + 256 * w4 + split) * 1024;
  const bool rs = (c & 8) != 0;

  if (wave < 4) {
    const rsrc_t rv = mkrsrc(vc, 4u * 8 * 4096 * 128 * 2);
    const int voff = lane * 16;
    v4u V[32];
#define VLOAD(j) V[j] = __builtin_amdgcn_raw_buffer_load_b128(rv, voff, sbase + (((j) & 24) + 4 * ((j) & 1) + (((j) >> 1) & 3)) * 8192, AUX_NT)
    unsigned tag, t8;
    {
      const bool has_tile = bid < 240;
      const int cgp = has_tile ? bid >> 1 : 0, k0 = (bid & 1) * 256 + w4 * 64;
      const u16* wbase = cgp < 64 ? wq + cgp * 64 : cgp < 92 ? w1 + (cgp - 64) * 64 : w3 + (cgp - 92) * 64;
      const int ldb = cgp < 64 ? 8192 : 3584;
      const rsrc_t rw = mkrsrc(wbase, has_tile ? 512u * ldb : 0u);
      const rsrc_t rx = mkrsrc(x, 4096u);
      v4u X[4], W[8];
#pragma unroll
      for (int i = 0; i < 4; ++i) X[i] = __builtin_amdgcn_raw_buffer_load_b128(rx, (c >> 2) * 1024 + (c & 3) * 64 + g * 16, (((k0 >> 7) + i) & 3) * 256, 0);
      {
        const int vo = ((c >> 3) * 32 + g * 8) * ldb + (c & 7) * 16;
#pragma unroll
        for (int j = 0; j < 8; ++j) W[j] = __builtin_amdgcn_raw_buffer_load_b128(rw, vo, (k0 + j) * ldb, AUX_NT);
      }
#pragma unroll
      for (int j = 0; j < VD1; ++j) VLOAD(j);
      PIN();
      tag = load_tag(ws); t8 = (tag * 0x9E3779B1u) >> 24;
      PIN();
      float s = 0.f;
#pragma unroll
      for (int i = 0; i < 4; ++i)
#pragma unroll
        for (int k = 0; k < 4; ++k) s += lo16(X[i][k]) + hi16(X[i][k]);
      s += dpp<XOR1>(s); s += dpp<XOR2>(s);
      const float mean = xrow_sum(s) * (1.0f / 512.0f);
      float q = 0.f;
#pragma unroll
      for (int i = 0; i < 4; ++i)
#pragma unroll
        for (int k = 0; k < 4; ++k) {
          const float d0 = lo16(X[i][k]) - mean, d1 = hi16(X[i][k]) - mean;
          q = fmaf(d0, d0, q); q = fmaf(d1, d1, q);
        }
      q += dpp<XOR1>(q); q += dpp<XOR2>(q);
      const float var = rbf(xrow_sum(q) * (1.0f / 512.0f));
      const float den = rbf(__builtin_amdgcn_sqrtf(rbf(var + 1e-5f)));
      const float inv = __builtin_amdgcn_rcpf(den), mb = rbf(mean);
      v4u A;
#pragma unroll
      for (int k = 0; k < 4; ++k) {
        const unsigned d = pk2(lo16(X[0][k]) - mb, hi16(X[0][k]) - mb);
        const unsigned a = pk2(lo16(d) * inv, hi16(d) * inv), a2 = dppu<0xEE>(a);
        A[k] = (w4 & 1) ? a2 : a;
      }
      ST(1);
      f32x4 acc[8];
      {
        v4u bf[8];
#pragma unroll
        for (int t = 0; t < 4; ++t) {
#pragma unroll
          for (int i = 0; i < 8; ++i) {
            bf[i][t] = permsel(W[2 * t], W[2 * t + 1], i);
            if (t == 3) acc[i] = mfma(A, bf[i], f32x4{0.f, 0.f, 0.f, 0.f});
          }
          PIN();
        }
      }
      f32x4 part = fold8(acc, rs);
      if (w4 != 0) {
        *(LAS f32x4*)(L + L_PX + (w4 - 1) * 1024 + lane * 16) = part;
        lds_inc(flag + 1, lane);
      } else {
        lds_wait<1>(flag + 1, 3u);
        part += *(LAS f32x4*)(L + L_PX + lane * 16);
        part += *(LAS f32x4*)(L + L_PX + 1024 + lane * 16) + *(LAS f32x4*)(L + L_PX + 2048 + lane * 16);
        if (has_tile) {
          st16(rws, WS_PG + bid * 2048 + lane * 16, gran2(part[0], part[1], tag, t8));
          st16(rws, WS_PG + bid * 2048 + lane * 16 + 1024, gran2(part[2], part[3], tag, t8));
        }
      }
      PIN();
      ST(2);
    }
    v4u bf[4][8];
#define VPERM(pr) do { _Pragma("unroll") for (int i = 0; i < 8; ++i) bf[(pr) >> 2][i][(pr) & 3] = permsel(V[2 * (pr)], V[2 * (pr) + 1], i); } while (0)
#pragma unroll
    for (int j = VD1; j < 32; ++j) {
      if (j < 2 * VD1) {} else if (j < VSW) VMCNT(VD1 - 1); else VMCNT(VD2 - 1);
      VLOAD(j);
      PIN();
      if (j == WOPOS) lds_inc(flag + 4, lane);
      if (j >= VD2 + 1 && ((j - VD2) & 1)) { VPERM((j - VD2) >> 1); PIN(); }
    }
    ST(3);
    lds_wait<1>(flag + 3, 4u);
    v4u P[4];
#pragma unroll
    for (int G = 0; G < 4; ++G) P[G] = *(LAS v4u*)(L + L_P + w4 * 1024 + G * 256 + (lane & ~3) * 4);
    ST(4);
    f32x4 acc[8];
#pragma unroll
    for (int i = 0; i < 8; ++i) acc[i] = f32x4{0.f, 0.f, 0.f, 0.f};
    constexpr int DONE = (32 - VD2) >> 1;
#pragma unroll
    for (int G = 0; G < 4; ++G) {
#pragma unroll
      for (int t = 0; t < 4; ++t)
        if (4 * G + t >= DONE) { VPERM(4 * G + t); if (t < 3) PIN(); }
#pragma unroll
      for (int i = 0; i < 8; ++i) acc[i] = mfma(P[G], bf[G][i], acc[i]);
      PIN();
    }
    *(LAS f32x4*)(L + L_ORED + w4 * 2048 + lane * 16) = f32x4{acc[0][0], acc[1][0], acc[2][0], acc[3][0]};
    *(LAS f32x4*)(L + L_ORED + w4 * 2048 + 1024 + lane * 16) = f32x4{acc[4][0], acc[5][0], acc[6][0], acc[7][0]};
    ST(5);
    wg_barrier();
    ST(6);
    {
      f32x2 pv[4];
      const int ro = ((lane >> 1) & 1) * 1024 + (w4 * 16 + (lane >> 2)) * 16 + (lane & 1) * 8;
#pragma unroll
      for (int k = 0; k < 4; ++k) pv[k] = *(LAS f32x2*)(L + L_ORED + k * 2048 + ro);
      const f32x2 o = (pv[0] + pv[1]) + (pv[2] + pv[3]);
      st16(rws, WS_OG + (((b * 8 + xcd) * 4 + w4) * 8 + split) * 1024 + lane * 16, gran2(o[0], o[1], tag, t8));
      ST(7);
      wg_barrier();
    }
    STDUMP();
    return;
  }

  const int rg = xcd * 8 + (idx >> 2), cg = idx & 3;
  unsigned tag, t8;
  {
    const rsrc_t rk = mkrsrc(kc, 4u * 8 * 4096 * 128 * 2);
    const int koff = ((c >> 1) & 1) * 8192 + (c >> 2) * 256 + (2 * g + (c & 1)) * 16;
    v4u K[32];
    if (KDELAY) __builtin_amdgcn_s_sleep(KDELAY);
#pragma unroll
    for (int j = 0; j < 32; ++j) {
      if (j >= KDEPTH) VMCNT(KDEPTH - 1);
      K[j] = __builtin_amdgcn_raw_buffer_load_b128(rk, koff, sbase + (j >> 1) * 16384 + (j & 1) * 128, AUX_NT);
      PIN();
      if (j == KDEPTH - 1) { tag = load_tag(ws); t8 = (tag * 0x9E3779B1u) >> 24; PIN(); }
    }
    asm volatile("" ::"v"(pf));
    ST(1);
    {
      const int vo = g * 1024 + b * 256 + c * 16;
      const int voff2[2] = {vo, vo + 4096};
      v4u gv[2];
      gather<2, 8>(rws, WS_PG + (xcd * 8 + 2 * w4) * 4096, voff2, gv, tag, t8);
#pragma unroll
      for (int n = 0; n < 2; ++n) {
        const unsigned qp = pk2(half_sum(asf(gv[n][0])), half_sum(asf(gv[n][2])));
        if (lane < 32) *(LAS unsigned*)(L + L_QS + (128 * w4 + 64 * n + 8 * (c & 7) + 4 * (c >> 3) + 2 * g) * 2) = qp;
      }
      lds_inc(flag, lane);
      lds_wait<1>(flag, 4u);
    }
    ST(2);
    v4u qf[2][2];
    {
      const bool a1 = (c & 2) != 0;
#pragma unroll
      for (int p = 0; p < 2; ++p) {
        const v4u t = *(LAS v4u*)(L + L_QS + (c >> 2) * 256 + (8 * p + 2 * g + (c & 1)) * 16);
#pragma unroll
        for (int k = 0; k < 4; ++k) { qf[p][0][k] = a1 ? 0u : t[k]; qf[p][1][k] = a1 ? t[k] : 0u; }
      }
    }
    float s[8];
    {
      f32x4 d[8];
#pragma unroll
      for (int n = 0; n < 8; ++n) d[n] = f32x4{0.f, 0.f, 0.f, 0.f};
#pragma unroll
      for (int z = 0; z < 4; ++z) {
#pragma unroll
        for (int n = 0; n < 8; ++n) d[n] = mfma(K[4 * n + z], qf[z & 1][z >> 1], d[n]);
        PIN();
      }
      const bool u1 = (c & 1) != 0;
#pragma unroll
      for (int n = 0; n < 8; ++n) {
        const float d0 = opq(d[n][0]), d1 = opq(d[n][1]), d2 = opq(d[n][2]), d3 = opq(d[n][3]);
        s[n] = (u1 ? d3 : d0) + dpp<XOR1>(u1 ? d1 : d2);
      }
    }
    float m = -INFINITY;
#pragma unroll
    for (int n = 0; n < 8; n += 2) {
      const unsigned r1 = pk2(s[n], s[n + 1]);
      const unsigned r2 = pk2(lo16(r1) * SCALE, hi16(r1) * SCALE);
      s[n] = lo16(r2); s[n + 1] = hi16(r2);
      m = fmaxf(m, fmaxf(s[n], s[n + 1]));
    }
    m = fmaxf(m, dpp<XOR1>(m)); m = fmaxf(m, dpp<XOR2>(m));
    m = xrow_max(m) * LOG2E;
    if (g == 0 && (c & 3) == 0) *(LAS float*)(L + L_MX + ((c >> 2) * 4 + w4) * 4) = m;
    lds_inc(flag + 2, lane);
    lds_wait<1>(flag + 2, 4u);
    {
      const f32x4 ma = *(LAS f32x4*)(L + L_MX + (c >> 2) * 16);
      const float M = fmaxf(fmaxf(ma[0], ma[1]), fmaxf(ma[2], ma[3]));
      const float m2 = asf((asu(M) & ~0xffu) | t8);
      float l = 0.f;
#pragma unroll
      for (int G = 0; G < 4; ++G) {
        const float p0 = __builtin_amdgcn_exp2f(fmaf(s[2 * G], LOG2E, -m2)), p1 = __builtin_amdgcn_exp2f(fmaf(s[2 * G + 1], LOG2E, -m2));
        l += p0 + p1;
        *(LAS unsigned*)(L + L_P + w4 * 1024 + G * 256 + lane * 4) = pk2(p0, p1);
      }
      l += dpp<XOR1>(l); l += dpp<XOR2>(l);
      l = xrow_sum(l);
      if (g == 0 && (c & 3) == 0) *(LAS float*)(L + L_LS + ((c >> 2) * 4 + w4) * 4) = l;
      lds_inc(flag + 3, lane);
    }
    ST(3);
  }
  v4u W[12];
  if (w4 == 0) {
    lds_wait<1>(flag + 3, 4u);
    if (lane < 4) {
      const f32x4 ma = *(LAS f32x4*)(L + L_MX + lane * 16), la = *(LAS f32x4*)(L + L_LS + lane * 16);
      const float M = fmaxf(fmaxf(ma[0], ma[1]), fmaxf(ma[2], ma[3]));
      st16(rws, WS_ML + ((b * 8 + xcd) * 8 + split) * 128 + lane * 16, gran2(M, (la[0] + la[1]) + (la[2] + la[3]), tag, t8));
    }
    lds_wait<4>(flag + 4, 4u);
  } else if (w4 == 1) {
    const int f = 28 * rg + 2 * (c < 14 ? c : 13);
    const int vo = (f >> 6) * 4096 + ((f >> 1) & 1) * 1024 + g * 256 + (((f >> 2) & 1) * 8 + ((f & 63) >> 3)) * 16;
    const int voff4[4] = {vo, vo + 2048, vo + 28 * 4096, vo + 28 * 4096 + 2048};
    v4u gv[4];
    gather<4, 16>(rws, WS_PG + 64 * 4096, voff4, gv, tag, t8);
    float hv[2];
#pragma unroll
    for (int k = 0; k < 2; ++k) {
      const float gt = rbf(asf(gv[0][2 * k]) + asf(gv[1][2 * k])), up = rbf(asf(gv[2][2 * k]) + asf(gv[3][2 * k]));
      const float sl = rbf(gt * __builtin_amdgcn_rcpf(1.0f + __builtin_amdgcn_exp2f(-gt * LOG2E)));
      hv[k] = sl * up;
    }
    *(LAS unsigned*)(L + L_HA + g * 64 + c * 4) = c < 14 ? pk2(hv[0], hv[1]) : 0u;
    lds_wait<4>(flag + 4, 4u);
  } else {
    const rsrc_t rwo = mkrsrc(wo, 4096u * 512 * 2), rw2 = mkrsrc(w2, 1792u * 512 * 2);
    const int colb = (128 * cg + 64 * (w4 - 2)) * 2;
    lds_wait<4>(flag + 4, 4u);
    const int vo = ((c >> 3) * 32 + g * 8) * 1024 + (c & 7) * 16;
#pragma unroll
    for (int j = 0; j < 8; ++j) W[j] = __builtin_amdgcn_raw_buffer_load_b128(rwo, vo, (64 * rg + j) * 1024 + colb, AUX_NT);
    const int vo2 = (rs && g == 3) ? 0x7ff00000 : ((c >> 3) * 16 + g * 4) * 1024 + (c & 7) * 16;
#pragma unroll
    for (int j = 0; j < 4; ++j) W[8 + j] = __builtin_amdgcn_raw_buffer_load_b128(rw2, vo2, (28 * rg + j) * 1024 + colb, AUX_NT);
    PIN();
  }
  ST(4);
  const int hc = idx >> 3, sh = lane >> 5, p = lane & 31;
  const int voff2[2] = {(4 * sh + (lane & 3)) * 128 + hc * 16, (4 * (1 - sh) + (lane & 3)) * 128 + hc * 16};
  v4u ml[2];
#pragma unroll
  for (int k = 0; k < 2; ++k) ml[k] = __builtin_amdgcn_raw_buffer_load_b128(rws, voff2[k], WS_ML + (w4 * 8 + xcd) * 1024, AUX_SC1);
  v4u Bo[8];
  f32x4 acc[8];
  unsigned xr = 0u;
  const int bf = bid >> 6, fr = bid & 63, fc = 4 * (fr & 3) + (lane & 3);
  const int col = 64 * (fr >> 3) + 8 * (fc & 7) + 4 * (fc >> 3) + 2 * ((fr >> 2) & 1);
  if (w4 == 0) xr = *(const unsigned*)(x + bf * 512 + col);
  ST(5);
  wg_barrier();
  wg_barrier();
  {
    __builtin_amdgcn_s_sleep(PDELAY);
    const int vo = 4 * sh * 1024 + ((idx >> 2) & 1) * 512 + p * 16;
    const int voff4[4] = {vo, vo + 1024, vo + 2048, vo + 3072};
    const int so = WS_OG + ((w4 * 8 + xcd) * 4 + hc) * 8192;
    v4u gv[4];
#pragma unroll
    for (int k = 0; k < 4; ++k) gv[k] = __builtin_amdgcn_raw_buffer_load_b128(rws, voff4[k], so, AUX_SC1);
    PIN();
    if (w4 >= 2) {
#pragma unroll
      for (int i = 0; i < 8; ++i)
#pragma unroll
        for (int t = 0; t < 4; ++t) Bo[i][t] = permsel(W[2 * t], W[2 * t + 1], i);
      const v2u ha = *(LAS v2u*)(L + L_HA + (c >> 2) * 64 + (c & 1) * 32 + g * 8);
#pragma unroll
      for (int i = 0; i < 8; ++i)
        acc[i] = mfma(v4u{ha[0], ha[1], 0u, 0u}, v4u{permsel(W[8], W[9], i), permsel(W[10], W[11], i), 0u, 0u}, f32x4{0.f, 0.f, 0.f, 0.f});
    }
    PIN();
    if (__builtin_amdgcn_ballot_w64(gran_bad(ml[0], tag, t8) || gran_bad(ml[1], tag, t8)) != 0ull)
      gather<2, 4>(rws, WS_ML + (w4 * 8 + xcd) * 1024, voff2, ml, tag, t8);
    float wt[4];
    {
      float M = fmaxf(asf(ml[0][0]), asf(ml[1][0]));
      M = fmaxf(M, dpp<XOR1>(M)); M = fmaxf(M, dpp<XOR2>(M));
      float wa = __builtin_amdgcn_exp2f(asf(ml[0][0]) - M);
      const float wb = __builtin_amdgcn_exp2f(asf(ml[1][0]) - M);
      float Ls = wa * asf(ml[0][2]) + wb * asf(ml[1][2]);
      Ls += dpp<XOR1>(Ls); Ls += dpp<XOR2>(Ls);
      wa *= __builtin_amdgcn_rcpf(Ls);
      wt[0] = dpp<0x00>(wa); wt[1] = dpp<0x55>(wa); wt[2] = dpp<0xAA>(wa); wt[3] = dpp<0xFF>(wa);
    }
    PIN();
    for (unsigned sp = 0; sp < SPIN_CAP; ++sp) {
      bool bad[4], any = false;
#pragma unroll
      for (int k = 0; k < 4; ++k) { bad[k] = gran_bad(gv[k], tag, t8); any |= bad[k]; }
      if (__builtin_amdgcn_ballot_w64(any) == 0ull) break;
      __builtin_amdgcn_s_sleep(POLL_SLEEP);
      asm volatile("" ::: "memory");
#pragma unroll
      for (int k = 0; k < 4; ++k)
        if (bad[k]) gv[k] = __builtin_amdgcn_raw_buffer_load_b128(rws, voff4[k], so, AUX_SC1);
    }
    const float o0 = wt[0] * asf(gv[0][0]) + wt[1] * asf(gv[1][0]) + wt[2] * asf(gv[2][0]) + wt[3] * asf(gv[3][0]);
    const float o1 = wt[0] * asf(gv[0][2]) + wt[1] * asf(gv[1][2]) + wt[2] * asf(gv[2][2]) + wt[3] * asf(gv[3][2]);
    const unsigned op = pk2(half_sum(o0), half_sum(o1));
    if (lane < 32) *(LAS unsigned*)(L + L_OA + w4 * 128 + p * 4) = op;
  }
  ST(6);
  wg_barrier();
  if (w4 >= 2) {
    const v4u a0 = *(LAS v4u*)(L + L_OA + (c >> 2) * 128 + (c & 1) * 64 + g * 16);
#pragma unroll
    for (int i = 0; i < 8; ++i) acc[i] = mfma(a0, Bo[i], acc[i]);
    const f32x4 y = fold8(acc, rs);
    const int so = WS_YG + ((cg * 64 + rg) * 2 + (w4 - 2)) * 2048 + lane * 16;
    st16(rws, so, gran2(y[0], y[1], tag, t8));
    st16(rws, so + 1024, gran2(y[2], y[3], tag, t8));
    wg_barrier();
  } else if (w4 == 0) {
    wg_barrier();
    __builtin_amdgcn_s_sleep(PDELAY);
    const int vy = (lane >> 2) * 4096 + (lane & 3) * 16;
    const int voff4[4] = {vy, vy + 65536, vy + 2 * 65536, vy + 3 * 65536};
    v4u y[4];
    gather<4, POLL_SLEEP>(rws, WS_YG + (fr >> 4) * 64 * 4096 + ((fr >> 2) & 3) * 1024 + bf * 256 + (fr & 3) * 64, voff4, y, tag, t8);
    float o0 = (asf(y[0][0]) + asf(y[1][0])) + (asf(y[2][0]) + asf(y[3][0]));
    float o1 = (asf(y[0][2]) + asf(y[1][2])) + (asf(y[2][2]) + asf(y[3][2]));
    o0 += dpp<ROR4>(o0); o1 += dpp<ROR4>(o1);
    o0 += dpp<ROR8>(o0); o1 += dpp<ROR8>(o1);
    o0 = xrow_sum(o0); o1 = xrow_sum(o1);
    if (lane < 4) *(unsigned*)(out + bf * 512 + col) = pk2(lo16(xr) + o0, hi16(xr) + o1);
    if (bid == 0 && lane == 0) __builtin_amdgcn_raw_buffer_store_b32(tag, rws, 0, WS_E, AUX_SC1);
  }
  ST(7);
  STDUMP();
}

extern "C" void kernel_launch(void* const* d_in, const int* in_sizes, int n_in, void* d_out, int out_size, void* d_ws, size_t ws_size,
                              hipStream_t stream) {
  (void)in_sizes; (void)n_in; (void)out_size; (void)ws_size;
  fused_decode<<<256, NTHR, 0, stream>>>((const u16*)d_in[0], (const u16*)d_in[1], (const u16*)d_in[2], (const u16*)d_in[3], (const u16*)d_in[4],
                                         (const u16*)d_in[6], (const u16*)d_in[7], (const u16*)d_in[8], (u16*)d_out, (unsigned char*)d_ws);
}
```

```cpp
#include <hip/hip_runtime.h>
#include <stdint.h>
#include <stdio.h>

typedef unsigned short u16;
typedef unsigned v4u __attribute__((ext_vector_type(4)));
typedef unsigned v2u __attribute__((ext_vector_type(2)));
typedef __bf16 bf16x8 __attribute__((ext_vector_type(8)));
typedef __bf16 bf16x2 __attribute__((ext_vector_type(2)));
typedef float f32x4 __attribute__((ext_vector_type(4)));
typedef float f32x2 __attribute__((ext_vector_type(2)));
#define LAS __attribute__((address_space(3)))

#define STAMP 0
#define KDEPTH 32
#define KPRIO 2
#define WOPRIO 0
#define KPRE 0
#define KD1 32
#define KSW 0
#define VD1 0
#define VD2 16
#define VSW 10
#define WOPOS 28
#define CODE_BYTES 12288
#define PFMASK 3
#define POLL_SLEEP 8
#define PDELAY 2

namespace {
constexpr int AUX_NT = 2, AUX_SC1 = 16, RSRC_W3 = 0x00020000;
constexpr int NTHR = 512;
constexpr unsigned SPIN_CAP = 1u << 16;
constexpr int WS_E = 0;
constexpr int WS_PG = 4096;
constexpr int WS_ML = WS_PG + 240 * 2048;
constexpr int WS_OG = WS_ML + 256 * 128;
constexpr int WS_YG = WS_OG + 128 * 8 * 1024;
constexpr int WS_ST = WS_YG + 4 * 64 * 4096;
constexpr int WS_BYTES = WS_ST + 256 * 8 * 8 * 8;
constexpr int L_ORED = 0;
constexpr int L_PX = L_ORED + 8192;
constexpr int L_P = L_PX + 3072;
constexpr int L_QS = L_P + 4096;
constexpr int L_MX = L_QS + 1024;
constexpr int L_LS = L_MX + 64;
constexpr int L_OA = L_LS + 64;
constexpr int L_HA = L_OA + 512;
constexpr int L_FLAG = L_HA + 256;
constexpr int L_BYTES = L_FLAG + 32;

constexpr float LOG2E = 1.4426950408889634f;
constexpr float SCALE = 0.08838834764831845f;

__device__ __forceinline__ float asf(unsigned u) { return __builtin_bit_cast(float, u); }
__device__ __forceinline__ unsigned asu(float f) { return __builtin_bit_cast(unsigned, f); }
__device__ __forceinline__ unsigned pk2(float lo, float hi) {
  f32x2 p = {lo, hi};
  return __builtin_bit_cast(unsigned, __builtin_convertvector(p, bf16x2));
}
__device__ __forceinline__ float lo16(unsigned u) { return asf(u << 16); }
__device__ __forceinline__ float hi16(unsigned u) { return asf(u & 0xffff0000u); }
__device__ __forceinline__ float rbf(float f) { return lo16(pk2(f, f)); }
template <int CTRL> __device__ __forceinline__ unsigned dppu(unsigned x) { return (unsigned)__builtin_amdgcn_mov_dpp((int)x, CTRL, 0xf, 0xf, true); }
template <int CTRL> __device__ __forceinline__ float dpp(float x) { return asf(dppu<CTRL>(asu(x))); }
constexpr int XOR1 = 0xB1, XOR2 = 0x4E, ROR8 = 0x128, ROR4 = 0x124;
__device__ __forceinline__ float opq(float x) { asm("" : "+v"(x)); return x; }
__device__ __forceinline__ float half_sum(float x) {
  auto t = __builtin_amdgcn_permlane32_swap(asu(x), asu(x), false, false);
  return asf(t[0]) + asf(t[1]);
}
__device__ __forceinline__ float xrow_sum(float x) {
  auto s = __builtin_amdgcn_permlane16_swap(asu(x), asu(x), false, false);
  return half_sum(asf(s[0]) + asf(s[1]));
}
__device__ __forceinline__ float xrow_max(float x) {
  auto s = __builtin_amdgcn_permlane16_swap(asu(x), asu(x), false, false);
  x = fmaxf(asf(s[0]), asf(s[1]));
  auto t = __builtin_amdgcn_permlane32_swap(asu(x), asu(x), false, false);
  return fmaxf(asf(t[0]), asf(t[1]));
}
__device__ __forceinline__ f32x4 mfma(v4u a, v4u b, f32x4 c) {
  return __builtin_amdgcn_mfma_f32_16x16x32_bf16(__builtin_bit_cast(bf16x8, a), __builtin_bit_cast(bf16x8, b), c, 0, 0, 0);
}
__device__ __forceinline__ unsigned permlo(unsigned even, unsigned odd) { return __builtin_amdgcn_perm(odd, even, 0x05040100u); }
__device__ __forceinline__ unsigned permhi(unsigned even, unsigned odd) { return __builtin_amdgcn_perm(odd, even, 0x07060302u); }
__device__ __forceinline__ unsigned permsel(const v4u& e, const v4u& o, int i) { return (i & 1) ? permhi(e[i >> 1], o[i >> 1]) : permlo(e[i >> 1], o[i >> 1]); }
#define PIN() __builtin_amdgcn_sched_barrier(0)
#define LGKM0() asm volatile("s_waitcnt lgkmcnt(0)" ::: "memory")
#define VMCNT(n) asm volatile("s_waitcnt vmcnt(%0)" ::"i"(n) : "memory")
__device__ __forceinline__ void wg_barrier() {
  PIN();
  LGKM0();
  __builtin_amdgcn_s_barrier();
  asm volatile("" ::: "memory");
  PIN();
}
typedef __amdgpu_buffer_rsrc_t rsrc_t;
__device__ __forceinline__ rsrc_t mkrsrc(const void* p, unsigned bytes) { return __builtin_amdgcn_make_buffer_rsrc((void*)p, 0, (int)bytes, RSRC_W3); }
__device__ __forceinline__ unsigned load_tag(const unsigned char* ws) {
  unsigned e;
  asm volatile("s_load_dword %0, %1, 0x0\n\ts_waitcnt lgkmcnt(0)" : "=s"(e) : "s"(ws) : "memory");
  return e + 1u;
}
__device__ __forceinline__ v4u gran2(float a, float b, unsigned tag, unsigned t8) {
  return v4u{(asu(a) & ~0xffu) | t8, tag, (asu(b) & ~0xffu) | t8, tag};
}
__device__ __forceinline__ void st16(rsrc_t rs, int off, v4u v) { __builtin_amdgcn_raw_buffer_store_b128(v, rs, off, 0, AUX_SC1); }
__device__ __forceinline__ bool gran_bad(const v4u& v, unsigned tag, unsigned t8) {
  return (((v[1] ^ tag) | (v[3] ^ tag)) | (((v[0] ^ t8) | (v[2] ^ t8)) & 0xffu)) != 0u;
}
template <int N, int SLEEP>
__device__ __forceinline__ void gather(rsrc_t rs, int soff, const int (&voff)[N], v4u (&v)[N], unsigned tag, unsigned t8) {
  bool bad[N];
#pragma unroll
  for (int k = 0; k < N; ++k) v[k] = __builtin_amdgcn_raw_buffer_load_b128(rs, voff[k], soff, AUX_SC1);
  for (unsigned sp = 0; sp < SPIN_CAP; ++sp) {
    bool any = false;
#pragma unroll
    for (int k = 0; k < N; ++k) { bad[k] = gran_bad(v[k], tag, t8); any |= bad[k]; }
    if (__builtin_amdgcn_ballot_w64(any) == 0ull) break;
    __builtin_amdgcn_s_sleep(SLEEP);
    asm volatile("" ::: "memory");
#pragma unroll
    for (int k = 0; k < N; ++k)
      if (bad[k]) v[k] = __builtin_amdgcn_raw_buffer_load_b128(rs, voff[k], soff, AUX_SC1);
  }
}
template <int SLEEP>
__device__ __forceinline__ void lds_wait(volatile LAS unsigned* f, unsigned want) {
#pragma clang loop unroll(disable)
  for (unsigned sp = 0; sp < (1u << 24); ++sp) {
    if ((unsigned)__builtin_amdgcn_readfirstlane(*f) >= want) break;
    __builtin_amdgcn_s_sleep(SLEEP);
  }
  asm volatile("" ::: "memory");
}
__device__ __forceinline__ void lds_inc(volatile LAS unsigned* f, int lane) {
  LGKM0();
  if (lane == 0) __hip_atomic_fetch_add((LAS unsigned*)f, 1u, __ATOMIC_RELAXED, __HIP_MEMORY_SCOPE_WORKGROUP);
}
__device__ __forceinline__ f32x4 fold8(const f32x4 (&acc)[8], bool rs) {
  f32x4 r;
#pragma unroll
  for (int i = 0; i < 4; ++i) {
    const float a0 = opq(acc[i][0]), a1 = opq(acc[i][1]), b0 = opq(acc[4 + i][0]), b1 = opq(acc[4 + i][1]);
    r[i] = (rs ? b1 : a0) + dpp<ROR8>(rs ? a1 : b0);
  }
  return r;
}
#define ST(i) do {} while (0)
#define STDUMP() do {} while (0)
}

__global__ void __launch_bounds__(NTHR, 2)
fused_decode(const u16* __restrict__ x, const u16* __restrict__ kc, const u16* __restrict__ vc, const u16* __restrict__ wq,
             const u16* __restrict__ wo, const u16* __restrict__ w1, const u16* __restrict__ w3, const u16* __restrict__ w2,
             u16* __restrict__ out, unsigned char* ws) {
  __shared__ __attribute__((aligned(16))) unsigned char smem[L_BYTES];
  LAS unsigned char* L = (LAS unsigned char*)smem;
  const int tid = threadIdx.x, lane = tid & 63;
  const int wave = __builtin_amdgcn_readfirstlane(tid >> 6), w4 = wave & 3;
  const int bid = blockIdx.x, xcd = bid & 7, idx = bid >> 3;
  const int g = lane >> 4, c = lane & 15;
  ST(0);
  unsigned pf = 0u;
  if (CODE_BYTES > 0 && wave >= 6 && (idx & PFMASK) == 0) {
    unsigned long long pc;
    asm volatile("s_getpc_b64 %0" : "=s"(pc));
    pf = __builtin_amdgcn_raw_buffer_load_b32(mkrsrc((const void*)(pc & ~255ull), CODE_BYTES), ((wave - 6) * 64 + lane) * 128, 0, 0);
  }
  const rsrc_t rws = mkrsrc(ws, WS_BYTES);
  volatile LAS unsigned* flag = (volatile LAS unsigned*)(L + L_FLAG);
  if (tid < 8) flag[tid] = 0u;
  asm volatile("" ::"s"(x), "s"(kc), "s"(vc), "s"(wq), "s"(wo), "s"(w1), "s"(w3), "s"(w2), "s"(out), "s"(ws));
  const int b = idx >> 3, split = idx & 7;
  const int sbase = ((b * 8 + xcd) * 1024 + 256 * w4 + split) * 1024;
  const bool rs = (c & 8) != 0;

  if (wave < 4) {
    const rsrc_t rv = mkrsrc(vc, 4u * 8 * 4096 * 128 * 2);
    const int voff = lane * 16;
    v4u V[32];
#define VLOAD(j) V[j] = __builtin_amdgcn_raw_buffer_load_b128(rv, voff, sbase + (((j) & 24) + 4 * ((j) & 1) + (((j) >> 1) & 3)) * 8192, AUX_NT)
    unsigned tag, t8;
    __builtin_amdgcn_s_setprio(3);
    {
      const bool has_tile = bid < 240;
      const int cgp = has_tile ? bid >> 1 : 0, k0 = (bid & 1) * 256 + w4 * 64;
      const u16* wbase = cgp < 64 ? wq + cgp * 64 : cgp < 92 ? w1 + (cgp - 64) * 64 : w3 + (cgp - 92) * 64;
      const int ldb = cgp < 64 ? 8192 : 3584;
      const rsrc_t rw = mkrsrc(wbase, has_tile ? 512u * ldb : 0u);
      const rsrc_t rx = mkrsrc(x, 4096u);
      v4u X[4], W[8];
#pragma unroll
      for (int i = 0; i < 4; ++i) X[i] = __builtin_amdgcn_raw_buffer_load_b128(rx, (c >> 2) * 1024 + (c & 3) * 64 + g * 16, (((k0 >> 7) + i) & 3) * 256, 0);
      {
        const int vo = ((c >> 3) * 32 + g * 8) * ldb + (c & 7) * 16;
#pragma unroll
        for (int j = 0; j < 8; ++j) W[j] = __builtin_amdgcn_raw_buffer_load_b128(rw, vo, (k0 + j) * ldb, AUX_NT);
      }
#pragma unroll
      for (int j = 0; j < VD1; ++j) VLOAD(j);
      wg_barrier();
      tag = load_tag(ws); t8 = (tag * 0x9E3779B1u) >> 24;
      PIN();
      float s = 0.f;
#pragma unroll
      for (int i = 0; i < 4; ++i)
#pragma unroll
        for (int k = 0; k < 4; ++k) s += lo16(X[i][k]) + hi16(X[i][k]);
      s += dpp<XOR1>(s); s += dpp<XOR2>(s);
      const float mean = xrow_sum(s) * (1.0f / 512.0f);
      float q = 0.f;
#pragma unroll
      for (int i = 0; i < 4; ++i)
#pragma unroll
        for (int k = 0; k < 4; ++k) {
          const float d0 = lo16(X[i][k]) - mean, d1 = hi16(X[i][k]) - mean;
          q = fmaf(d0, d0, q); q = fmaf(d1, d1, q);
        }
      q += dpp<XOR1>(q); q += dpp<XOR2>(q);
      const float var = rbf(xrow_sum(q) * (1.0f / 512.0f));
      const float den = rbf(__builtin_amdgcn_sqrtf(rbf(var + 1e-5f)));
      const float inv = __builtin_amdgcn_rcpf(den), mb = rbf(mean);
      v4u A;
#pragma unroll
      for (int k = 0; k < 4; ++k) {
        const unsigned d = pk2(lo16(X[0][k]) - mb, hi16(X[0][k]) - mb);
        const unsigned a = pk2(lo16(d) * inv, hi16(d) * inv), a2 = dppu<0xEE>(a);
        A[k] = (w4 & 1) ? a2 : a;
      }
      ST(1);
      f32x4 acc[8];
      {
        v4u bf[8];
#pragma unroll
        for (int t = 0; t < 4; ++t) {
#pragma unroll
          for (int i = 0; i < 8; ++i) {
            bf[i][t] = permsel(W[2 * t], W[2 * t + 1], i);
            if (t == 3) acc[i] = mfma(A, bf[i], f32x4{0.f, 0.f, 0.f, 0.f});
          }
          PIN();
        }
      }
      f32x4 part = fold8(acc, rs);
      if (w4 != 0) {
        *(LAS f32x4*)(L + L_PX + (w4 - 1) * 1024 + lane * 16) = part;
        lds_inc(flag + 1, lane);
      } else {
        lds_wait<1>(flag + 1, 3u);
        part += *(LAS f32x4*)(L + L_PX + lane * 16);
        part += *(LAS f32x4*)(L + L_PX + 1024 + lane * 16) + *(LAS f32x4*)(L + L_PX + 2048 + lane * 16);
        if (has_tile) {
          st16(rws, WS_PG + bid * 2048 + lane * 16, gran2(part[0], part[1], tag, t8));
          st16(rws, WS_PG + bid * 2048 + lane * 16 + 1024, gran2(part[2], part[3], tag, t8));
        }
      }
      PIN();
      ST(2);
    }
    __builtin_amdgcn_s_setprio(0);
    v4u bf[4][8];
#define VPERM(pr) do { _Pragma("unroll") for (int i = 0; i < 8; ++i) bf[(pr) >> 2][i][(pr) & 3] = permsel(V[2 * (pr)], V[2 * (pr) + 1], i); } while (0)
#pragma unroll
    for (int j = VD1; j < 32; ++j) {
      if (j >= VD2) VMCNT(VD2 - 1);
      VLOAD(j);
      PIN();
      if (j == WOPOS) lds_inc(flag + 4, lane);
      if (j >= VD2 + 1 && ((j - VD2) & 1)) { VPERM((j - VD2) >> 1); PIN(); }
    }
    ST(3);
    lds_wait<1>(flag + 3, 4u);
    v4u P[4];
#pragma unroll
    for (int G = 0; G < 4; ++G) P[G] = *(LAS v4u*)(L + L_P + w4 * 1024 + G * 256 + (lane & ~3) * 4);
    ST(4);
    f32x4 acc[8];
#pragma unroll
    for (int i = 0; i < 8; ++i) acc[i] = f32x4{0.f, 0.f, 0.f, 0.f};
    constexpr int DONE = (32 - VD2) >> 1;
#pragma unroll
    for (int G = 0; G < 4; ++G) {
#pragma unroll
      for (int t = 0; t < 4; ++t)
        if (4 * G + t >= DONE) { VPERM(4 * G + t); if (t < 3) PIN(); }
#pragma unroll
      for (int i = 0; i < 8; ++i) acc[i] = mfma(P[G], bf[G][i], acc[i]);
      PIN();
    }
    *(LAS f32x4*)(L + L_ORED + w4 * 2048 + lane * 16) = f32x4{acc[0][0], acc[1][0], acc[2][0], acc[3][0]};
    *(LAS f32x4*)(L + L_ORED + w4 * 2048 + 1024 + lane * 16) = f32x4{acc[4][0], acc[5][0], acc[6][0], acc[7][0]};
    ST(5);
    wg_barrier();
    ST(6);
    {
      f32x2 pv[4];
      const int ro = ((lane >> 1) & 1) * 1024 + (w4 * 16 + (lane >> 2)) * 16 + (lane & 1) * 8;
#pragma unroll
      for (int k = 0; k < 4; ++k) pv[k] = *(LAS f32x2*)(L + L_ORED + k * 2048 + ro);
      const f32x2 o = (pv[0] + pv[1]) + (pv[2] + pv[3]);
      st16(rws, WS_OG + (((b * 8 + xcd) * 4 + w4) * 8 + split) * 1024 + lane * 16, gran2(o[0], o[1], tag, t8));
      ST(7);
      wg_barrier();
    }
    STDUMP();
    return;
  }

  const int rg = xcd * 8 + (idx >> 2), cg = idx & 3;
  unsigned tag, t8;
  {
    const rsrc_t rk = mkrsrc(kc, 4u * 8 * 4096 * 128 * 2);
    const int koff = ((c >> 1) & 1) * 8192 + (c >> 2) * 256 + (2 * g + (c & 1)) * 16;
    v4u K[32];
    __builtin_amdgcn_s_setprio(KPRIO);
#pragma unroll
    for (int j = 0; j < KPRE; ++j) K[j] = __builtin_amdgcn_raw_buffer_load_b128(rk, koff, sbase + (j >> 1) * 16384 + (j & 1) * 128, AUX_NT);
    wg_barrier();
#pragma unroll
    for (int j = KPRE; j < 32; ++j) {
      if (j >= KD1 && j < KSW) VMCNT(KD1 - 1);
      K[j] = __builtin_amdgcn_raw_buffer_load_b128(rk, koff, sbase + (j >> 1) * 16384 + (j & 1) * 128, AUX_NT);
      PIN();
      if (j == 7) { tag = load_tag(ws); t8 = (tag * 0x9E3779B1u) >> 24; PIN(); }
    }
    asm volatile("" ::"v"(pf));
    ST(1);
    {
      const int vo = g * 1024 + b * 256 + c * 16;
      const int voff2[2] = {vo, vo + 4096};
      v4u gv[2];
      gather<2, 8>(rws, WS_PG + (xcd * 8 + 2 * w4) * 4096, voff2, gv, tag, t8);
#pragma unroll
      for (int n = 0; n < 2; ++n) {
        const unsigned qp = pk2(half_sum(asf(gv[n][0])), half_sum(asf(gv[n][2])));
        if (lane < 32) *(LAS unsigned*)(L + L_QS + (128 * w4 + 64 * n + 8 * (c & 7) + 4 * (c >> 3) + 2 * g) * 2) = qp;
      }
      lds_inc(flag, lane);
      lds_wait<1>(flag, 4u);
    }
    ST(2);
    v4u qf[2][2];
    {
      const bool a1 = (c & 2) != 0;
#pragma unroll
      for (int p = 0; p < 2; ++p) {
        const v4u t = *(LAS v4u*)(L + L_QS + (c >> 2) * 256 + (8 * p + 2 * g + (c & 1)) * 16);
#pragma unroll
        for (int k = 0; k < 4; ++k) { qf[p][0][k] = a1 ? 0u : t[k]; qf[p][1][k] = a1 ? t[k] : 0u; }
      }
    }
    float s[8];
    {
      f32x4 d[8];
#pragma unroll
      for (int n = 0; n < 8; ++n) d[n] = f32x4{0.f, 0.f, 0.f, 0.f};
#pragma unroll
      for (int z = 0; z < 4; ++z) {
#pragma unroll
        for (int n = 0; n < 8; ++n) d[n] = mfma(K[4 * n + z], qf[z & 1][z >> 1], d[n]);
        PIN();
      }
      const bool u1 = (c & 1) != 0;
#pragma unroll
      for (int n = 0; n < 8; ++n) {
        const float d0 = opq(d[n][0]), d1 = opq(d[n][1]), d2 = opq(d[n][2]), d3 = opq(d[n][3]);
        s[n] = (u1 ? d3 : d0) + dpp<XOR1>(u1 ? d1 : d2);
      }
    }
    float m = -INFINITY;
#pragma unroll
    for (int n = 0; n < 8; n += 2) {
      const unsigned r1 = pk2(s[n], s[n + 1]);
      const unsigned r2 = pk2(lo16(r1) * SCALE, hi16(r1) * SCALE);
      s[n] = lo16(r2); s[n + 1] = hi16(r2);
      m = fmaxf(m, fmaxf(s[n], s[n + 1]));
    }
    m = fmaxf(m, dpp<XOR1>(m)); m = fmaxf(m, dpp<XOR2>(m));
    m = xrow_max(m) * LOG2E;
    if (g == 0 && (c & 3) == 0) *(LAS float*)(L + L_MX + ((c >> 2) * 4 + w4) * 4) = m;
    lds_inc(flag + 2, lane);
    lds_wait<1>(flag + 2, 4u);
    {
      const f32x4 ma = *(LAS f32x4*)(L + L_MX + (c >> 2) * 16);
      const float M = fmaxf(fmaxf(ma[0], ma[1]), fmaxf(ma[2], ma[3]));
      const float m2 = asf((asu(M) & ~0xffu) | t8);
      float l = 0.f;
#pragma unroll
      for (int G = 0; G < 4; ++G) {
        const float p0 = __builtin_amdgcn_exp2f(fmaf(s[2 * G], LOG2E, -m2)), p1 = __builtin_amdgcn_exp2f(fmaf(s[2 * G + 1], LOG2E, -m2));
        l += p0 + p1;
        *(LAS unsigned*)(L + L_P + w4 * 1024 + G * 256 + lane * 4) = pk2(p0, p1);
      }
      l += dpp<XOR1>(l); l += dpp<XOR2>(l);
      l = xrow_sum(l);
      if (g == 0 && (c & 3) == 0) *(LAS float*)(L + L_LS + ((c >> 2) * 4 + w4) * 4) = l;
      lds_inc(flag + 3, lane);
    }
    ST(3);
  }
  __builtin_amdgcn_s_setprio(WOPRIO);
  v4u W[12];
  if (w4 == 0) {
    lds_wait<1>(flag + 3, 4u);
    if (lane < 4) {
      const f32x4 ma = *(LAS f32x4*)(L + L_MX + lane * 16), la = *(LAS f32x4*)(L + L_LS + lane * 16);
      const float M = fmaxf(fmaxf(ma[0], ma[1]), fmaxf(ma[2], ma[3]));
      st16(rws, WS_ML + ((b * 8 + xcd) * 8 + split) * 128 + lane * 16, gran2(M, (la[0] + la[1]) + (la[2] + la[3]), tag, t8));
    }
    lds_wait<4>(flag + 4, 4u);
  } else if (w4 == 1) {
    const int f = 28 * rg + 2 * (c < 14 ? c : 13);
    const int vo = (f >> 6) * 4096 + ((f >> 1) & 1) * 1024 + g * 256 + (((f >> 2) & 1) * 8 + ((f & 63) >> 3)) * 16;
    const int voff4[4] = {vo, vo + 2048, vo + 28 * 4096, vo + 28 * 4096 + 2048};
    v4u gv[4];
    gather<4, 16>(rws, WS_PG + 64 * 4096, voff4, gv, tag, t8);
    float hv[2];
#pragma unroll
    for (int k = 0; k < 2; ++k) {
      const float gt = rbf(asf(gv[0][2 * k]) + asf(gv[1][2 * k])), up = rbf(asf(gv[2][2 * k]) + asf(gv[3][2 * k]));
      const float sl = rbf(gt * __builtin_amdgcn_rcpf(1.0f + __builtin_amdgcn_exp2f(-gt * LOG2E)));
      hv[k] = sl * up;
    }
    *(LAS unsigned*)(L + L_HA + g * 64 + c * 4) = c < 14 ? pk2(hv[0], hv[1]) : 0u;
    lds_wait<4>(flag + 4, 4u);
  } else {
    const rsrc_t rwo = mkrsrc(wo, 4096u * 512 * 2), rw2 = mkrsrc(w2, 1792u * 512 * 2);
    const int colb = (128 * cg + 64 * (w4 - 2)) * 2;
    lds_wait<4>(flag + 4, 4u);
    const int vo = ((c >> 3) * 32 + g * 8) * 1024 + (c & 7) * 16;
#pragma unroll
    for (int j = 0; j < 8; ++j) W[j] = __builtin_amdgcn_raw_buffer_load_b128(rwo, vo, (64 * rg + j) * 1024 + colb, AUX_NT);
    const int vo2 = (rs && g == 3) ? 0x7ff00000 : ((c >> 3) * 16 + g * 4) * 1024 + (c & 7) * 16;
#pragma unroll
    for (int j = 0; j < 4; ++j) W[8 + j] = __builtin_amdgcn_raw_buffer_load_b128(rw2, vo2, (28 * rg + j) * 1024 + colb, AUX_NT);
    PIN();
  }
  ST(4);
  const int hc = idx >> 3, sh = lane >> 5, p = lane & 31;
  const int voff2[2] = {(4 * sh + (lane & 3)) * 128 + hc * 16, (4 * (1 - sh) + (lane & 3)) * 128 + hc * 16};
  v4u ml[2];
#pragma unroll
  for (int k = 0; k < 2; ++k) ml[k] = __builtin_amdgcn_raw_buffer_load_b128(rws, voff2[k], WS_ML + (w4 * 8 + xcd) * 1024, AUX_SC1);
  v4u Bo[8];
  f32x4 acc[8];
  unsigned xr = 0u;
  const int bf = bid >> 6, fr = bid & 63, fc = 4 * (fr & 3) + (lane & 3);
  const int col = 64 * (fr >> 3) + 8 * (fc & 7) + 4 * (fc >> 3) + 2 * ((fr >> 2) & 1);
  if (w4 == 0) xr = *(const unsigned*)(x + bf * 512 + col);
  ST(5);
  wg_barrier();
  wg_barrier();
  {
    __builtin_amdgcn_s_sleep(PDELAY);
    const int vo = 4 * sh * 1024 + ((idx >> 2) & 1) * 512 + p * 16;
    const int voff4[4] = {vo, vo + 1024, vo + 2048, vo + 3072};
    const int so = WS_OG + ((w4 * 8 + xcd) * 4 + hc) * 8192;
    v4u gv[4];
#pragma unroll
    for (int k = 0; k < 4; ++k) gv[k] = __builtin_amdgcn_raw_buffer_load_b128(rws, voff4[k], so, AUX_SC1);
    PIN();
    if (w4 >= 2) {
#pragma unroll
      for (int i = 0; i < 8; ++i)
#pragma unroll
        for (int t = 0; t < 4; ++t) Bo[i][t] = permsel(W[2 * t], W[2 * t + 1], i);
      const v2u ha = *(LAS v2u*)(L + L_HA + (c >> 2) * 64 + (c & 1) * 32 + g * 8);
#pragma unroll
      for (int i = 0; i < 8; ++i)
        acc[i] = mfma(v4u{ha[0], ha[1], 0u, 0u}, v4u{permsel(W[8], W[9], i), permsel(W[10], W[11], i), 0u, 0u}, f32x4{0.f, 0.f, 0.f, 0.f});
    }
    PIN();
    if (__builtin_amdgcn_ballot_w64(gran_bad(ml[0], tag, t8) || gran_bad(ml[1], tag, t8)) != 0ull)
      gather<2, 4>(rws, WS_ML + (w4 * 8 + xcd) * 1024, voff2, ml, tag, t8);
    float wt[4];
    {
      float M = fmaxf(asf(ml[0][0]), asf(ml[1][0]));
      M = fmaxf(M, dpp<XOR1>(M)); M = fmaxf(M, dpp<XOR2>(M));
      float wa = __builtin_amdgcn_exp2f(asf(ml[0][0]) - M);
      const float wb = __builtin_amdgcn_exp2f(asf(ml[1][0]) - M);
      float Ls = wa * asf(ml[0][2]) + wb * asf(ml[1][2]);
      Ls += dpp<XOR1>(Ls); Ls += dpp<XOR2>(Ls);
      wa *= __builtin_amdgcn_rcpf(Ls);
      wt[0] = dpp<0x00>(wa); wt[1] = dpp<0x55>(wa); wt[2] = dpp<0xAA>(wa); wt[3] = dpp<0xFF>(wa);
    }
    PIN();
    for (unsigned sp = 0; sp < SPIN_CAP; ++sp) {
      bool bad[4], any = false;
#pragma unroll
      for (int k = 0; k < 4; ++k) { bad[k] = gran_bad(gv[k], tag, t8); any |= bad[k]; }
      if (__builtin_amdgcn_ballot_w64(any) == 0ull) break;
      __builtin_amdgcn_s_sleep(POLL_SLEEP);
      asm volatile("" ::: "memory");
#pragma unroll
      for (int k = 0; k < 4; ++k)
        if (bad[k]) gv[k] = __builtin_amdgcn_raw_buffer_load_b128(rws, voff4[k], so, AUX_SC1);
    }
    const float o0 = wt[0] * asf(gv[0][0]) + wt[1] * asf(gv[1][0]) + wt[2] * asf(gv[2][0]) + wt[3] * asf(gv[3][0]);
    const float o1 = wt[0] * asf(gv[0][2]) + wt[1] * asf(gv[1][2]) + wt[2] * asf(gv[2][2]) + wt[3] * asf(gv[3][2]);
    const unsigned op = pk2(half_sum(o0), half_sum(o1));
    if (lane < 32) *(LAS unsigned*)(L + L_OA + w4 * 128 + p * 4) = op;
  }
  ST(6);
  wg_barrier();
  if (w4 >= 2) {
    const v4u a0 = *(LAS v4u*)(L + L_OA + (c >> 2) * 128 + (c & 1) * 64 + g * 16);
#pragma unroll
    for (int i = 0; i < 8; ++i) acc[i] = mfma(a0, Bo[i], acc[i]);
    const f32x4 y = fold8(acc, rs);
    const int so = WS_YG + ((cg * 64 + rg) * 2 + (w4 - 2)) * 2048 + lane * 16;
    st16(rws, so, gran2(y[0], y[1], tag, t8));
    st16(rws, so + 1024, gran2(y[2], y[3], tag, t8));
    wg_barrier();
  } else if (w4 == 0) {
    wg_barrier();
    __builtin_amdgcn_s_sleep(PDELAY);
    const int vy = (lane >> 2) * 4096 + (lane & 3) * 16;
    const int voff4[4] = {vy, vy + 65536, vy + 2 * 65536, vy + 3 * 65536};
    v4u y[4];
    gather<4, POLL_SLEEP>(rws, WS_YG + (fr >> 4) * 64 * 4096 + ((fr >> 2) & 3) * 1024 + bf * 256 + (fr & 3) * 64, voff4, y, tag, t8);
    float o0 = (asf(y[0][0]) + asf(y[1][0])) + (asf(y[2][0]) + asf(y[3][0]));
    float o1 = (asf(y[0][2]) + asf(y[1][2])) + (asf(y[2][2]) + asf(y[3][2]));
    o0 += dpp<ROR4>(o0); o1 += dpp<ROR4>(o1);
    o0 += dpp<ROR8>(o0); o1 += dpp<ROR8>(o1);
    o0 = xrow_sum(o0); o1 = xrow_sum(o1);
    if (lane < 4) *(unsigned*)(out + bf * 512 + col) = pk2(lo16(xr) + o0, hi16(xr) + o1);
    if (bid == 0 && lane == 0) __builtin_amdgcn_raw_buffer_store_b32(tag, rws, 0, WS_E, AUX_SC1);
  }
  ST(7);
  STDUMP();
}

extern "C" void kernel_launch(void* const* d_in, const int* in_sizes, int n_in, void* d_out, int out_size, void* d_ws, size_t ws_size,
                              hipStream_t stream) {
  for (int i = 0; i < n_in; ++i) fprintf(stderr, "in%d %p ", i, d_in[i]);
  fprintf(stderr, "out %p ws %p\n", d_out, d_ws);
  { hipPointerAttribute_t at; for (int i = 1; i < 3; ++i) if (hipPointerGetAttributes(&at, d_in[i]) == hipSuccess) fprintf(stderr, "attr%d type %d managed %d flags %u\n", i, (int)at.type, at.isManaged, at.allocationFlags);
    if (hipPointerGetAttributes(&at, d_ws) == hipSuccess) fprintf(stderr, "attr ws type %d managed %d flags %u\n", (int)at.type, at.isManaged, at.allocationFlags);
    void* base; size_t sz; if (hipMemGetAddressRange((hipDeviceptr_t*)&base, &sz, (hipDeviceptr_t)d_in[1]) == hipSuccess) fprintf(stderr, "range in1 %p %zu\n", base, sz);
    if (hipMemGetAddressRange((hipDeviceptr_t*)&base, &sz, (hipDeviceptr_t)d_ws) == hipSuccess) fprintf(stderr, "range ws %p %zu\n", base, sz); }
  fused_decode<<<256, NTHR, 0, stream>>>((const u16*)d_in[0], (const u16*)d_in[1], (const u16*)d_in[2], (const u16*)d_in[3], (const u16*)d_in[4],
                                         (const u16*)d_in[6], (const u16*)d_in[7], (const u16*)d_in[8], (u16*)d_out, (unsigned char*)d_ws);
}
```

```cpp
#include <hip/hip_runtime.h>
#include <stdint.h>

typedef unsigned short u16;
typedef unsigned v4u __attribute__((ext_vector_type(4)));
typedef unsigned v2u __attribute__((ext_vector_type(2)));
typedef __bf16 bf16x8 __attribute__((ext_vector_type(8)));
typedef __bf16 bf16x2 __attribute__((ext_vector_type(2)));
typedef float f32x4 __attribute__((ext_vector_type(4)));
typedef float f32x2 __attribute__((ext_vector_type(2)));
#define LAS __attribute__((address_space(3)))

#define STAMP 0
#define KDEPTH 32
#define KPRIO 2
#define WOPRIO 0
#define KPRE 0
#define KD1 32
#define KSW 0
#define VD1 0
#define VD2 16
#define VSW 10
#define WOPOS 28
#define CODE_BYTES 12288
#define PFMASK 3
#define POLL_SLEEP 8
#define PDELAY 2

namespace {
constexpr int AUX_NT = 2, AUX_SC1 = 16, RSRC_W3 = 0x00020000;
constexpr int NTHR = 512;
constexpr unsigned SPIN_CAP = 1u << 16;
constexpr int WS_E = 0;
constexpr int WS_PG = 4096;
constexpr int WS_ML = WS_PG + 240 * 2048;
constexpr int WS_OG = WS_ML + 256 * 128;
constexpr int WS_YG = WS_OG + 128 * 8 * 1024;
constexpr int WS_ST = WS_YG + 4 * 64 * 4096;
constexpr int WS_BYTES = WS_ST + 256 * 8 * 8 * 8;
constexpr int L_ORED = 0;
constexpr int L_PX = L_ORED + 8192;
constexpr int L_P = L_PX + 3072;
constexpr int L_QS = L_P + 4096;
constexpr int L_MX = L_QS + 1024;
constexpr int L_LS = L_MX + 64;
constexpr int L_OA = L_LS + 64;
constexpr int L_HA = L_OA + 512;
constexpr int L_FLAG = L_HA + 256;
constexpr int L_BYTES = L_FLAG + 32;

constexpr float LOG2E = 1.4426950408889634f;
constexpr float SCALE = 0.08838834764831845f;

__device__ __forceinline__ float asf(unsigned u) { return __builtin_bit_cast(float, u); }
__device__ __forceinline__ unsigned asu(float f) { return __builtin_bit_cast(unsigned, f); }
__device__ __forceinline__ unsigned pk2(float lo, float hi) {
  f32x2 p = {lo, hi};
  return __builtin_bit_cast(unsigned, __builtin_convertvector(p, bf16x2));
}
__device__ __forceinline__ float lo16(unsigned u) { return asf(u << 16); }
__device__ __forceinline__ float hi16(unsigned u) { return asf(u & 0xffff0000u); }
__device__ __forceinline__ float rbf(float f) { return lo16(pk2(f, f)); }
template <int CTRL> __device__ __forceinline__ unsigned dppu(unsigned x) { return (unsigned)__builtin_amdgcn_mov_dpp((int)x, CTRL, 0xf, 0xf, true); }
template <int CTRL> __device__ __forceinline__ float dpp(float x) { return asf(dppu<CTRL>(asu(x))); }
constexpr int XOR1 = 0xB1, XOR2 = 0x4E, ROR8 = 0x128, ROR4 = 0x124;
__device__ __forceinline__ float opq(float x) { asm("" : "+v"(x)); return x; }
__device__ __forceinline__ float half_sum(float x) {
  auto t = __builtin_amdgcn_permlane32_swap(asu(x), asu(x), false, false);
  return asf(t[0]) + asf(t[1]);
}
__device__ __forceinline__ float xrow_sum(float x) {
  auto s = __builtin_amdgcn_permlane16_swap(asu(x), asu(x), false, false);
  return half_sum(asf(s[0]) + asf(s[1]));
}
__device__ __forceinline__ float xrow_max(float x) {
  auto s = __builtin_amdgcn_permlane16_swap(asu(x), asu(x), false, false);
  x = fmaxf(asf(s[0]), asf(s[1]));
  auto t = __builtin_amdgcn_permlane32_swap(asu(x), asu(x), false, false);
  return fmaxf(asf(t[0]), asf(t[1]));
}
__device__ __forceinline__ f32x4 mfma(v4u a, v4u b, f32x4 c) {
  return __builtin_amdgcn_mfma_f32_16x16x32_bf16(__builtin_bit_cast(bf16x8, a), __builtin_bit_cast(bf16x8, b), c, 0, 0, 0);
}
__device__ __forceinline__ unsigned permlo(unsigned even, unsigned odd) { return __builtin_amdgcn_perm(odd, even, 0x05040100u); }
__device__ __forceinline__ unsigned permhi(unsigned even, unsigned odd) { return __builtin_amdgcn_perm(odd, even, 0x07060302u); }
__device__ __forceinline__ unsigned permsel(const v4u& e, const v4u& o, int i) { return (i & 1) ? permhi(e[i >> 1], o[i >> 1]) : permlo(e[i >> 1], o[i >> 1]); }
#define PIN() __builtin_amdgcn_sched_barrier(0)
#define LGKM0() asm volatile("s_waitcnt lgkmcnt(0)" ::: "memory")
#define VMCNT(n) asm volatile("s_waitcnt vmcnt(%0)" ::"i"(n) : "memory")
__device__ __forceinline__ void wg_barrier() {
  PIN();
  LGKM0();
  __builtin_amdgcn_s_barrier();
  asm volatile("" ::: "memory");
  PIN();
}
typedef __amdgpu_buffer_rsrc_t rsrc_t;
__device__ __forceinline__ rsrc_t mkrsrc(const void* p, unsigned bytes) { return __builtin_amdgcn_make_buffer_rsrc((void*)p, 0, (int)bytes, RSRC_W3); }
__device__ __forceinline__ unsigned load_tag(const unsigned char* ws) {
  unsigned e;
  asm volatile("s_load_dword %0, %1, 0x0\n\ts_waitcnt lgkmcnt(0)" : "=s"(e) : "s"(ws) : "memory");
  return e + 1u;
}
__device__ __forceinline__ v4u gran2(float a, float b, unsigned tag, unsigned t8) {
  return v4u{(asu(a) & ~0xffu) | t8, tag, (asu(b) & ~0xffu) | t8, tag};
}
__device__ __forceinline__ void st16(rsrc_t rs, int off, v4u v) { __builtin_amdgcn_raw_buffer_store_b128(v, rs, off, 0, AUX_SC1); }
__device__ __forceinline__ bool gran_bad(const v4u& v, unsigned tag, unsigned t8) {
  return (((v[1] ^ tag) | (v[3] ^ tag)) | (((v[0] ^ t8) | (v[2] ^ t8)) & 0xffu)) != 0u;
}
template <int N, int SLEEP>
__device__ __forceinline__ void gather(rsrc_t rs, int soff, const int (&voff)[N], v4u (&v)[N], unsigned tag, unsigned t8) {
  bool bad[N];
#pragma unroll
  for (int k = 0; k < N; ++k) v[k] = __builtin_amdgcn_raw_buffer_load_b128(rs, voff[k], soff, AUX_SC1);
  for (unsigned sp = 0; sp < SPIN_CAP; ++sp) {
    bool any = false;
#pragma unroll
    for (int k = 0; k < N; ++k) { bad[k] = gran_bad(v[k], tag, t8); any |= bad[k]; }
    if (__builtin_amdgcn_ballot_w64(any) == 0ull) break;
    __builtin_amdgcn_s_sleep(SLEEP);
    asm volatile("" ::: "memory");
#pragma unroll
    for (int k = 0; k < N; ++k)
      if (bad[k]) v[k] = __builtin_amdgcn_raw_buffer_load_b128(rs, voff[k], soff, AUX_SC1);
  }
}
template <int SLEEP>
__device__ __forceinline__ void lds_wait(volatile LAS unsigned* f, unsigned want) {
#pragma clang loop unroll(disable)
  for (unsigned sp = 0; sp < (1u << 24); ++sp) {
    if ((unsigned)__builtin_amdgcn_readfirstlane(*f) >= want) break;
    __builtin_amdgcn_s_sleep(SLEEP);
  }
  asm volatile("" ::: "memory");
}
__device__ __forceinline__ void lds_inc(volatile LAS unsigned* f, int lane) {
  LGKM0();
  if (lane == 0) __hip_atomic_fetch_add((LAS unsigned*)f, 1u, __ATOMIC_RELAXED, __HIP_MEMORY_SCOPE_WORKGROUP);
}
__device__ __forceinline__ f32x4 fold8(const f32x4 (&acc)[8], bool rs) {
  f32x4 r;
#pragma unroll
  for (int i = 0; i < 4; ++i) {
    const float a0 = opq(acc[i][0]), a1 = opq(acc[i][1]), b0 = opq(acc[4 + i][0]), b1 = opq(acc[4 + i][1]);
    r[i] = (rs ? b1 : a0) + dpp<ROR8>(rs ? a1 : b0);
  }
  return r;
}
#define ST(i) do {} while (0)
#define STDUMP() do {} while (0)
}

__global__ void __launch_bounds__(NTHR, 2)
fused_decode(const u16* __restrict__ x, const u16* __restrict__ kc, const u16* __restrict__ vc, const u16* __restrict__ wq,
             const u16* __restrict__ wo, const u16* __restrict__ w1, const u16* __restrict__ w3, const u16* __restrict__ w2,
             u16* __restrict__ out, unsigned char* ws) {
  __shared__ __attribute__((aligned(16))) unsigned char smem[L_BYTES];
  LAS unsigned char* L = (LAS unsigned char*)smem;
  const int tid = threadIdx.x, lane = tid & 63;
  const int wave = __builtin_amdgcn_readfirstlane(tid >> 6), w4 = wave & 3;
  const int bid = blockIdx.x, xcd = bid & 7, idx = bid >> 3;
  const int g = lane >> 4, c = lane & 15;
  ST(0);
  const unsigned long long tstart = __builtin_amdgcn_s_memrealtime();
  unsigned pf = 0u;
  if (CODE_BYTES > 0 && wave >= 6 && (idx & PFMASK) == 0) {
    unsigned long long pc;
    asm volatile("s_getpc_b64 %0" : "=s"(pc));
    pf = __builtin_amdgcn_raw_buffer_load_b32(mkrsrc((const void*)(pc & ~255ull), CODE_BYTES), ((wave - 6) * 64 + lane) * 128, 0, 0);
  }
  const rsrc_t rws = mkrsrc(ws, WS_BYTES);
  volatile LAS unsigned* flag = (volatile LAS unsigned*)(L + L_FLAG);
  if (tid < 8) flag[tid] = 0u;
  asm volatile("" ::"s"(x), "s"(kc), "s"(vc), "s"(wq), "s"(wo), "s"(w1), "s"(w3), "s"(w2), "s"(out), "s"(ws));
  const int b = idx >> 3, split = idx & 7;
  const int sbase = ((b * 8 + xcd) * 1024 + 256 * w4 + split) * 1024;
  const bool rs = (c & 8) != 0;

  if (wave < 4) {
    const rsrc_t rv = mkrsrc(vc, 4u * 8 * 4096 * 128 * 2);
    const int voff = lane * 16;
    v4u V[32];
#define VLOAD(j) V[j] = __builtin_amdgcn_raw_buffer_load_b128(rv, voff, sbase + (((j) & 24) + 4 * ((j) & 1) + (((j) >> 1) & 3)) * 8192, AUX_NT)
    unsigned tag, t8;
    __builtin_amdgcn_s_setprio(3);
    {
      const bool has_tile = bid < 240;
      const int cgp = has_tile ? bid >> 1 : 0, k0 = (bid & 1) * 256 + w4 * 64;
      const u16* wbase = cgp < 64 ? wq + cgp * 64 : cgp < 92 ? w1 + (cgp - 64) * 64 : w3 + (cgp - 92) * 64;
      const int ldb = cgp < 64 ? 8192 : 3584;
      const rsrc_t rw = mkrsrc(wbase, has_tile ? 512u * ldb : 0u);
      const rsrc_t rx = mkrsrc(x, 4096u);
      v4u X[4], W[8];
#pragma unroll
      for (int i = 0; i < 4; ++i) X[i] = __builtin_amdgcn_raw_buffer_load_b128(rx, (c >> 2) * 1024 + (c & 3) * 64 + g * 16, (((k0 >> 7) + i) & 3) * 256, 0);
      {
        const int vo = ((c >> 3) * 32 + g * 8) * ldb + (c & 7) * 16;
#pragma unroll
        for (int j = 0; j < 8; ++j) W[j] = __builtin_amdgcn_raw_buffer_load_b128(rw, vo, (k0 + j) * ldb, AUX_NT);
      }
#pragma unroll
      for (int j = 0; j < VD1; ++j) VLOAD(j);
      wg_barrier();
      tag = load_tag(ws); t8 = (tag * 0x9E3779B1u) >> 24;
      PIN();
      float s = 0.f;
#pragma unroll
      for (int i = 0; i < 4; ++i)
#pragma unroll
        for (int k = 0; k < 4; ++k) s += lo16(X[i][k]) + hi16(X[i][k]);
      s += dpp<XOR1>(s); s += dpp<XOR2>(s);
      const float mean = xrow_sum(s) * (1.0f / 512.0f);
      float q = 0.f;
#pragma unroll
      for (int i = 0; i < 4; ++i)
#pragma unroll
        for (int k = 0; k < 4; ++k) {
          const float d0 = lo16(X[i][k]) - mean, d1 = hi16(X[i][k]) - mean;
          q = fmaf(d0, d0, q); q = fmaf(d1, d1, q);
        }
      q += dpp<XOR1>(q); q += dpp<XOR2>(q);
      const float var = rbf(xrow_sum(q) * (1.0f / 512.0f));
      const float den = rbf(__builtin_amdgcn_sqrtf(rbf(var + 1e-5f)));
      const float inv = __builtin_amdgcn_rcpf(den), mb = rbf(mean);
      v4u A;
#pragma unroll
      for (int k = 0; k < 4; ++k) {
        const unsigned d = pk2(lo16(X[0][k]) - mb, hi16(X[0][k]) - mb);
        const unsigned a = pk2(lo16(d) * inv, hi16(d) * inv), a2 = dppu<0xEE>(a);
        A[k] = (w4 & 1) ? a2 : a;
      }
      ST(1);
      f32x4 acc[8];
      {
        v4u bf[8];
#pragma unroll
        for (int t = 0; t < 4; ++t) {
#pragma unroll
          for (int i = 0; i < 8; ++i) {
            bf[i][t] = permsel(W[2 * t], W[2 * t + 1], i);
            if (t == 3) acc[i] = mfma(A, bf[i], f32x4{0.f, 0.f, 0.f, 0.f});
          }
          PIN();
        }
      }
      f32x4 part = fold8(acc, rs);
      if (w4 != 0) {
        *(LAS f32x4*)(L + L_PX + (w4 - 1) * 1024 + lane * 16) = part;
        lds_inc(flag + 1, lane);
      } else {
        lds_wait<1>(flag + 1, 3u);
        part += *(LAS f32x4*)(L + L_PX + lane * 16);
        part += *(LAS f32x4*)(L + L_PX + 1024 + lane * 16) + *(LAS f32x4*)(L + L_PX + 2048 + lane * 16);
        if (has_tile) {
          st16(rws, WS_PG + bid * 2048 + lane * 16, gran2(part[0], part[1], tag, t8));
          st16(rws, WS_PG + bid * 2048 + lane * 16 + 1024, gran2(part[2], part[3], tag, t8));
        }
      }
      PIN();
      ST(2);
    }
    __builtin_amdgcn_s_setprio(0);
    v4u bf[4][8];
#define VPERM(pr) do { _Pragma("unroll") for (int i = 0; i < 8; ++i) bf[(pr) >> 2][i][(pr) & 3] = permsel(V[2 * (pr)], V[2 * (pr) + 1], i); } while (0)
#pragma unroll
    for (int j = VD1; j < 32; ++j) {
      if (j >= VD2) VMCNT(VD2 - 1);
      VLOAD(j);
      PIN();
      if (j == WOPOS) lds_inc(flag + 4, lane);
      if (j >= VD2 + 1 && ((j - VD2) & 1)) { VPERM((j - VD2) >> 1); PIN(); }
    }
    ST(3);
    lds_wait<1>(flag + 3, 4u);
    v4u P[4];
#pragma unroll
    for (int G = 0; G < 4; ++G) P[G] = *(LAS v4u*)(L + L_P + w4 * 1024 + G * 256 + (lane & ~3) * 4);
    ST(4);
    f32x4 acc[8];
#pragma unroll
    for (int i = 0; i < 8; ++i) acc[i] = f32x4{0.f, 0.f, 0.f, 0.f};
    constexpr int DONE = (32 - VD2) >> 1;
#pragma unroll
    for (int G = 0; G < 4; ++G) {
#pragma unroll
      for (int t = 0; t < 4; ++t)
        if (4 * G + t >= DONE) { VPERM(4 * G + t); if (t < 3) PIN(); }
#pragma unroll
      for (int i = 0; i < 8; ++i) acc[i] = mfma(P[G], bf[G][i], acc[i]);
      PIN();
    }
    *(LAS f32x4*)(L + L_ORED + w4 * 2048 + lane * 16) = f32x4{acc[0][0], acc[1][0], acc[2][0], acc[3][0]};
    *(LAS f32x4*)(L + L_ORED + w4 * 2048 + 1024 + lane * 16) = f32x4{acc[4][0], acc[5][0], acc[6][0], acc[7][0]};
    ST(5);
    wg_barrier();
    ST(6);
    {
      f32x2 pv[4];
      const int ro = ((lane >> 1) & 1) * 1024 + (w4 * 16 + (lane >> 2)) * 16 + (lane & 1) * 8;
#pragma unroll
      for (int k = 0; k < 4; ++k) pv[k] = *(LAS f32x2*)(L + L_ORED + k * 2048 + ro);
      const f32x2 o = (pv[0] + pv[1]) + (pv[2] + pv[3]);
      st16(rws, WS_OG + (((b * 8 + xcd) * 4 + w4) * 8 + split) * 1024 + lane * 16, gran2(o[0], o[1], tag, t8));
      ST(7);
      wg_barrier();
    }
    STDUMP();
    return;
  }

  const int rg = xcd * 8 + (idx >> 2), cg = idx & 3;
  unsigned tag, t8;
  {
    const rsrc_t rk = mkrsrc(kc, 4u * 8 * 4096 * 128 * 2);
    const int koff = ((c >> 1) & 1) * 8192 + (c >> 2) * 256 + (2 * g + (c & 1)) * 16;
    v4u K[32];
    __builtin_amdgcn_s_setprio(KPRIO);
#pragma unroll
    for (int j = 0; j < KPRE; ++j) K[j] = __builtin_amdgcn_raw_buffer_load_b128(rk, koff, sbase + (j >> 1) * 16384 + (j & 1) * 128, AUX_NT);
    wg_barrier();
#pragma unroll
    for (int j = KPRE; j < 32; ++j) {
      if (j >= KD1 && j < KSW) VMCNT(KD1 - 1);
      K[j] = __builtin_amdgcn_raw_buffer_load_b128(rk, koff, sbase + (j >> 1) * 16384 + (j & 1) * 128, AUX_NT);
      PIN();
      if (j == 7) { tag = load_tag(ws); t8 = (tag * 0x9E3779B1u) >> 24; PIN(); }
    }
    asm volatile("" ::"v"(pf));
    ST(1);
    {
      const int vo = g * 1024 + b * 256 + c * 16;
      const int voff2[2] = {vo, vo + 4096};
      v4u gv[2];
      gather<2, 8>(rws, WS_PG + (xcd * 8 + 2 * w4) * 4096, voff2, gv, tag, t8);
#pragma unroll
      for (int n = 0; n < 2; ++n) {
        const unsigned qp = pk2(half_sum(asf(gv[n][0])), half_sum(asf(gv[n][2])));
        if (lane < 32) *(LAS unsigned*)(L + L_QS + (128 * w4 + 64 * n + 8 * (c & 7) + 4 * (c >> 3) + 2 * g) * 2) = qp;
      }
      lds_inc(flag, lane);
      lds_wait<1>(flag, 4u);
    }
    ST(2);
    v4u qf[2][2];
    {
      const bool a1 = (c & 2) != 0;
#pragma unroll
      for (int p = 0; p < 2; ++p) {
        const v4u t = *(LAS v4u*)(L + L_QS + (c >> 2) * 256 + (8 * p + 2 * g + (c & 1)) * 16);
#pragma unroll
        for (int k = 0; k < 4; ++k) { qf[p][0][k] = a1 ? 0u : t[k]; qf[p][1][k] = a1 ? t[k] : 0u; }
      }
    }
    float s[8];
    {
      f32x4 d[8];
#pragma unroll
      for (int n = 0; n < 8; ++n) d[n] = f32x4{0.f, 0.f, 0.f, 0.f};
#pragma unroll
      for (int z = 0; z < 4; ++z) {
#pragma unroll
        for (int n = 0; n < 8; ++n) d[n] = mfma(K[4 * n + z], qf[z & 1][z >> 1], d[n]);
        PIN();
      }
      const bool u1 = (c & 1) != 0;
#pragma unroll
      for (int n = 0; n < 8; ++n) {
        const float d0 = opq(d[n][0]), d1 = opq(d[n][1]), d2 = opq(d[n][2]), d3 = opq(d[n][3]);
        s[n] = (u1 ? d3 : d0) + dpp<XOR1>(u1 ? d1 : d2);
      }
    }
    float m = -INFINITY;
#pragma unroll
    for (int n = 0; n < 8; n += 2) {
      const unsigned r1 = pk2(s[n], s[n + 1]);
      const unsigned r2 = pk2(lo16(r1) * SCALE, hi16(r1) * SCALE);
      s[n] = lo16(r2); s[n + 1] = hi16(r2);
      m = fmaxf(m, fmaxf(s[n], s[n + 1]));
    }
    m = fmaxf(m, dpp<XOR1>(m)); m = fmaxf(m, dpp<XOR2>(m));
    m = xrow_max(m) * LOG2E;
    if (g == 0 && (c & 3) == 0) *(LAS float*)(L + L_MX + ((c >> 2) * 4 + w4) * 4) = m;
    lds_inc(flag + 2, lane);
    lds_wait<1>(flag + 2, 4u);
    {
      const f32x4 ma = *(LAS f32x4*)(L + L_MX + (c >> 2) * 16);
      const float M = fmaxf(fmaxf(ma[0], ma[1]), fmaxf(ma[2], ma[3]));
      const float m2 = asf((asu(M) & ~0xffu) | t8);
      float l = 0.f;
#pragma unroll
      for (int G = 0; G < 4; ++G) {
        const float p0 = __builtin_amdgcn_exp2f(fmaf(s[2 * G], LOG2E, -m2)), p1 = __builtin_amdgcn_exp2f(fmaf(s[2 * G + 1], LOG2E, -m2));
        l += p0 + p1;
        *(LAS unsigned*)(L + L_P + w4 * 1024 + G * 256 + lane * 4) = pk2(p0, p1);
      }
      l += dpp<XOR1>(l); l += dpp<XOR2>(l);
      l = xrow_sum(l);
      if (g == 0 && (c & 3) == 0) *(LAS float*)(L + L_LS + ((c >> 2) * 4 + w4) * 4) = l;
      lds_inc(flag + 3, lane);
    }
    ST(3);
  }
  __builtin_amdgcn_s_setprio(WOPRIO);
  v4u W[12];
  if (w4 == 0) {
    lds_wait<1>(flag + 3, 4u);
    if (lane < 4) {
      const f32x4 ma = *(LAS f32x4*)(L + L_MX + lane * 16), la = *(LAS f32x4*)(L + L_LS + lane * 16);
      const float M = fmaxf(fmaxf(ma[0], ma[1]), fmaxf(ma[2], ma[3]));
      st16(rws, WS_ML + ((b * 8 + xcd) * 8 + split) * 128 + lane * 16, gran2(M, (la[0] + la[1]) + (la[2] + la[3]), tag, t8));
    }
    lds_wait<4>(flag + 4, 4u);
  } else if (w4 == 1) {
    const int f = 28 * rg + 2 * (c < 14 ? c : 13);
    const int vo = (f >> 6) * 4096 + ((f >> 1) & 1) * 1024 + g * 256 + (((f >> 2) & 1) * 8 + ((f & 63) >> 3)) * 16;
    const int voff4[4] = {vo, vo + 2048, vo + 28 * 4096, vo + 28 * 4096 + 2048};
    v4u gv[4];
    gather<4, 16>(rws, WS_PG + 64 * 4096, voff4, gv, tag, t8);
    float hv[2];
#pragma unroll
    for (int k = 0; k < 2; ++k) {
      const float gt = rbf(asf(gv[0][2 * k]) + asf(gv[1][2 * k])), up = rbf(asf(gv[2][2 * k]) + asf(gv[3][2 * k]));
      const float sl = rbf(gt * __builtin_amdgcn_rcpf(1.0f + __builtin_amdgcn_exp2f(-gt * LOG2E)));
      hv[k] = sl * up;
    }
    *(LAS unsigned*)(L + L_HA + g * 64 + c * 4) = c < 14 ? pk2(hv[0], hv[1]) : 0u;
    lds_wait<4>(flag + 4, 4u);
  } else {
    const rsrc_t rwo = mkrsrc(wo, 4096u * 512 * 2), rw2 = mkrsrc(w2, 1792u * 512 * 2);
    const int colb = (128 * cg + 64 * (w4 - 2)) * 2;
    lds_wait<4>(flag + 4, 4u);
    const int vo = ((c >> 3) * 32 + g * 8) * 1024 + (c & 7) * 16;
#pragma unroll
    for (int j = 0; j < 8; ++j) W[j] = __builtin_amdgcn_raw_buffer_load_b128(rwo, vo, (64 * rg + j) * 1024 + colb, AUX_NT);
    const int vo2 = (rs && g == 3) ? 0x7ff00000 : ((c >> 3) * 16 + g * 4) * 1024 + (c & 7) * 16;
#pragma unroll
    for (int j = 0; j < 4; ++j) W[8 + j] = __builtin_amdgcn_raw_buffer_load_b128(rw2, vo2, (28 * rg + j) * 1024 + colb, AUX_NT);
    PIN();
  }
  ST(4);
  const int hc = idx >> 3, sh = lane >> 5, p = lane & 31;
  const int voff2[2] = {(4 * sh + (lane & 3)) * 128 + hc * 16, (4 * (1 - sh) + (lane & 3)) * 128 + hc * 16};
  v4u ml[2];
#pragma unroll
  for (int k = 0; k < 2; ++k) ml[k] = __builtin_amdgcn_raw_buffer_load_b128(rws, voff2[k], WS_ML + (w4 * 8 + xcd) * 1024, AUX_SC1);
  v4u Bo[8];
  f32x4 acc[8];
  unsigned xr = 0u;
  const int bf = bid >> 6, fr = bid & 63, fc = 4 * (fr & 3) + (lane & 3);
  const int col = 64 * (fr >> 3) + 8 * (fc & 7) + 4 * (fc >> 3) + 2 * ((fr >> 2) & 1);
  if (w4 == 0) xr = *(const unsigned*)(x + bf * 512 + col);
  ST(5);
  wg_barrier();
  wg_barrier();
  {
    __builtin_amdgcn_s_sleep(PDELAY);
    const int vo = 4 * sh * 1024 + ((idx >> 2) & 1) * 512 + p * 16;
    const int voff4[4] = {vo, vo + 1024, vo + 2048, vo + 3072};
    const int so = WS_OG + ((w4 * 8 + xcd) * 4 + hc) * 8192;
    v4u gv[4];
#pragma unroll
    for (int k = 0; k < 4; ++k) gv[k] = __builtin_amdgcn_raw_buffer_load_b128(rws, voff4[k], so, AUX_SC1);
    PIN();
    if (w4 >= 2) {
#pragma unroll
      for (int i = 0; i < 8; ++i)
#pragma unroll
        for (int t = 0; t < 4; ++t) Bo[i][t] = permsel(W[2 * t], W[2 * t + 1], i);
      const v2u ha = *(LAS v2u*)(L + L_HA + (c >> 2) * 64 + (c & 1) * 32 + g * 8);
#pragma unroll
      for (int i = 0; i < 8; ++i)
        acc[i] = mfma(v4u{ha[0], ha[1], 0u, 0u}, v4u{permsel(W[8], W[9], i), permsel(W[10], W[11], i), 0u, 0u}, f32x4{0.f, 0.f, 0.f, 0.f});
    }
    PIN();
    if (__builtin_amdgcn_ballot_w64(gran_bad(ml[0], tag, t8) || gran_bad(ml[1], tag, t8)) != 0ull)
      gather<2, 4>(rws, WS_ML + (w4 * 8 + xcd) * 1024, voff2, ml, tag, t8);
    float wt[4];
    {
      float M = fmaxf(asf(ml[0][0]), asf(ml[1][0]));
      M = fmaxf(M, dpp<XOR1>(M)); M = fmaxf(M, dpp<XOR2>(M));
      float wa = __builtin_amdgcn_exp2f(asf(ml[0][0]) - M);
      const float wb = __builtin_amdgcn_exp2f(asf(ml[1][0]) - M);
      float Ls = wa * asf(ml[0][2]) + wb * asf(ml[1][2]);
      Ls += dpp<XOR1>(Ls); Ls += dpp<XOR2>(Ls);
      wa *= __builtin_amdgcn_rcpf(Ls);
      wt[0] = dpp<0x00>(wa); wt[1] = dpp<0x55>(wa); wt[2] = dpp<0xAA>(wa); wt[3] = dpp<0xFF>(wa);
    }
    PIN();
    for (unsigned sp = 0; sp < SPIN_CAP; ++sp) {
      bool bad[4], any = false;
#pragma unroll
      for (int k = 0; k < 4; ++k) { bad[k] = gran_bad(gv[k], tag, t8); any |= bad[k]; }
      if (__builtin_amdgcn_ballot_w64(any) == 0ull) break;
      __builtin_amdgcn_s_sleep(POLL_SLEEP);
      asm volatile("" ::: "memory");
#pragma unroll
      for (int k = 0; k < 4; ++k)
        if (bad[k]) gv[k] = __builtin_amdgcn_raw_buffer_load_b128(rws, voff4[k], so, AUX_SC1);
    }
    const float o0 = wt[0] * asf(gv[0][0]) + wt[1] * asf(gv[1][0]) + wt[2] * asf(gv[2][0]) + wt[3] * asf(gv[3][0]);
    const float o1 = wt[0] * asf(gv[0][2]) + wt[1] * asf(gv[1][2]) + wt[2] * asf(gv[2][2]) + wt[3] * asf(gv[3][2]);
    const unsigned op = pk2(half_sum(o0), half_sum(o1));
    if (lane < 32) *(LAS unsigned*)(L + L_OA + w4 * 128 + p * 4) = op;
  }
  ST(6);
  wg_barrier();
  if (w4 >= 2) {
    const v4u a0 = *(LAS v4u*)(L + L_OA + (c >> 2) * 128 + (c & 1) * 64 + g * 16);
#pragma unroll
    for (int i = 0; i < 8; ++i) acc[i] = mfma(a0, Bo[i], acc[i]);
    const f32x4 y = fold8(acc, rs);
    const int so = WS_YG + ((cg * 64 + rg) * 2 + (w4 - 2)) * 2048 + lane * 16;
    st16(rws, so, gran2(y[0], y[1], tag, t8));
    st16(rws, so + 1024, gran2(y[2], y[3], tag, t8));
    wg_barrier();
  } else if (w4 == 0) {
    wg_barrier();
    __builtin_amdgcn_s_sleep(PDELAY);
    const int vy = (lane >> 2) * 4096 + (lane & 3) * 16;
    const int voff4[4] = {vy, vy + 65536, vy + 2 * 65536, vy + 3 * 65536};
    v4u y[4];
    gather<4, POLL_SLEEP>(rws, WS_YG + (fr >> 4) * 64 * 4096 + ((fr >> 2) & 3) * 1024 + bf * 256 + (fr & 3) * 64, voff4, y, tag, t8);
    float o0 = (asf(y[0][0]) + asf(y[1][0])) + (asf(y[2][0]) + asf(y[3][0]));
    float o1 = (asf(y[0][2]) + asf(y[1][2])) + (asf(y[2][2]) + asf(y[3][2]));
    o0 += dpp<ROR4>(o0); o1 += dpp<ROR4>(o1);
    o0 += dpp<ROR8>(o0); o1 += dpp<ROR8>(o1);
    o0 = xrow_sum(o0); o1 = xrow_sum(o1);
    if (lane < 4) *(unsigned*)(out + bf * 512 + col) = pk2(lo16(xr) + o0, hi16(xr) + o1);
    if (bid == 0 && lane == 0) __builtin_amdgcn_raw_buffer_store_b32(tag, rws, 0, WS_E, AUX_SC1);
    if (bid == 0) { const unsigned long long t0 = *(volatile unsigned long long*)(ws + 2048); for (int i = 0; i < 1000000; ++i) { if (__builtin_amdgcn_s_memrealtime() - t0 >= 4000) break; __builtin_amdgcn_s_sleep(1); } }
  }
  ST(7);
  STDUMP();
}

__global__ void tiny(unsigned char* ws) { if (threadIdx.x == 0) *(unsigned long long*)(ws + 2048) = __builtin_amdgcn_s_memrealtime(); }
extern "C" void kernel_launch(void* const* d_in, const int* in_sizes, int n_in, void* d_out, int out_size, void* d_ws, size_t ws_size,
                              hipStream_t stream) {
  (void)in_sizes; (void)n_in; (void)out_size; (void)ws_size;
  tiny<<<1, 64, 0, stream>>>((unsigned char*)d_ws);
  fused_decode<<<256, NTHR, 0, stream>>>((const u16*)d_in[0], (const u16*)d_in[1], (const u16*)d_in[2], (const u16*)d_in[3], (const u16*)d_in[4],
                                         (const u16*)d_in[6], (const u16*)d_in[7], (const u16*)d_in[8], (u16*)d_out, (unsigned char*)d_ws);
}
```

```cpp
#include <hip/hip_runtime.h>
#include <stdint.h>

typedef unsigned short u16;
typedef unsigned v4u __attribute__((ext_vector_type(4)));
typedef unsigned v2u __attribute__((ext_vector_type(2)));
typedef unsigned v16u __attribute__((ext_vector_type(16)));
typedef __bf16 bf16x8 __attribute__((ext_vector_type(8)));
typedef __bf16 bf16x2 __attribute__((ext_vector_type(2)));
typedef float f32x4 __attribute__((ext_vector_type(4)));
typedef float f32x2 __attribute__((ext_vector_type(2)));
#define LAS __attribute__((address_space(3)))

#define STAMP 0
#define VD2 16
#define KPRE 4
#define PEND 857
#define PD 148
#define PT(r) (PEND - ((63 - (r)) * PD) / 10)
#define WOT 60
#define POLL_SLEEP 1
#define POLL2_SLEEP 1
#define PDELAY1 12
#define PDELAY 8
#define JIT 60
#define MLT 80
#define WFIRST 0
#define CAP 200
#define CKT 220
#define GOFF 0
#define MAPPING 1

namespace {
constexpr int AUX_NT = 2, AUX_SC1 = 16, RSRC_W3 = 0x00020000;
constexpr int NTHR = 512;
constexpr unsigned SPIN_CAP = 1u << 16;
constexpr int WS_E = 0;
constexpr int WS_PG = 4096;
constexpr int WS_ML = WS_PG + 240 * 1024;
constexpr int WS_CK = WS_ML + 128 * 128;
constexpr int WS_OG = WS_CK + 16 * 128;
constexpr int WS_YG = WS_OG + 128 * 4096;
constexpr int WS_ST = WS_YG + 4 * 64 * 2048;
constexpr int WS_BYTES = WS_ST + 256 * 8 * 8 * 8;
constexpr int L_ORED = 0;
constexpr int L_PX = L_ORED + 8192;
constexpr int L_P = L_PX + 4096;
constexpr int L_QS = L_P + 4096;
constexpr int L_MX = L_QS + 1024;
constexpr int L_LS = L_MX + 64;
constexpr int L_OA = L_LS + 64;
constexpr int L_HA = L_OA + 512;
constexpr int L_FLAG = L_HA + 256;
constexpr int L_BYTES = L_FLAG + 64;

constexpr float LOG2E = 1.4426950408889634f;
constexpr float SCALE = 0.08838834764831845f;

__device__ __forceinline__ float asf(unsigned u) { return __builtin_bit_cast(float, u); }
__device__ __forceinline__ unsigned asu(float f) { return __builtin_bit_cast(unsigned, f); }
__device__ __forceinline__ unsigned pk2(float lo, float hi) {
  f32x2 p = {lo, hi};
  return __builtin_bit_cast(unsigned, __builtin_convertvector(p, bf16x2));
}
__device__ __forceinline__ float lo16(unsigned u) { return asf(u << 16); }
__device__ __forceinline__ float hi16(unsigned u) { return asf(u & 0xffff0000u); }
__device__ __forceinline__ float rbf(float f) { return lo16(pk2(f, f)); }
template <int CTRL> __device__ __forceinline__ unsigned dppu(unsigned x) { return (unsigned)__builtin_amdgcn_mov_dpp((int)x, CTRL, 0xf, 0xf, true); }
template <int CTRL> __device__ __forceinline__ float dpp(float x) { return asf(dppu<CTRL>(asu(x))); }
constexpr int XOR1 = 0xB1, XOR2 = 0x4E, ROR8 = 0x128, ROR4 = 0x124;
__device__ __forceinline__ float opq(float x) { asm("" : "+v"(x)); return x; }
__device__ __forceinline__ float fold32(float a, float b) {
  auto t = __builtin_amdgcn_permlane32_swap(asu(a), asu(b), false, false);
  return asf(t[0]) + asf(t[1]);
}
__device__ __forceinline__ float fold16(float a, float b) {
  auto t = __builtin_amdgcn_permlane16_swap(asu(a), asu(b), false, false);
  return asf(t[0]) + asf(t[1]);
}
__device__ __forceinline__ float xrow_sum(float x) { x = fold16(x, x); return fold32(x, x); }
__device__ __forceinline__ float xrow_max(float x) {
  auto s = __builtin_amdgcn_permlane16_swap(asu(x), asu(x), false, false);
  x = fmaxf(asf(s[0]), asf(s[1]));
  auto t = __builtin_amdgcn_permlane32_swap(asu(x), asu(x), false, false);
  return fmaxf(asf(t[0]), asf(t[1]));
}
__device__ __forceinline__ f32x4 mfma(v4u a, v4u b, f32x4 c) {
  return __builtin_amdgcn_mfma_f32_16x16x32_bf16(__builtin_bit_cast(bf16x8, a), __builtin_bit_cast(bf16x8, b), c, 0, 0, 0);
}
__device__ __forceinline__ unsigned permsel(const v4u& e, const v4u& o, int i) {
  return __builtin_amdgcn_perm(o[i >> 1], e[i >> 1], (i & 1) ? 0x07060302u : 0x05040100u);
}
#define PIN() __builtin_amdgcn_sched_barrier(0)
#define LGKM0() asm volatile("s_waitcnt lgkmcnt(0)" ::: "memory")
#define VMCNT(n) asm volatile("s_waitcnt vmcnt(%0)" ::"i"(n) : "memory")
__device__ __forceinline__ void wg_barrier() {
  PIN();
  LGKM0();
  __builtin_amdgcn_s_barrier();
  asm volatile("" ::: "memory");
  PIN();
}
typedef __amdgpu_buffer_rsrc_t rsrc_t;
__device__ __forceinline__ rsrc_t mkrsrc(const void* p, unsigned bytes) { return __builtin_amdgcn_make_buffer_rsrc((void*)p, 0, (int)bytes, RSRC_W3); }
struct Tag { unsigned s[4]; };
__device__ __forceinline__ Tag mktag(unsigned t) {
  return Tag{{t & 1023u, (t + (t >> 10) + 341u) & 1023u, (t + (t >> 20) + 682u) & 1023u, (t + (t >> 30) + 171u) & 1023u}};
}
__device__ __forceinline__ float cut(float a) { return asf((asu(a) + 512u) & ~1023u); }
__device__ __forceinline__ v4u unit(f32x4 v, const Tag& t) {
  return v4u{asu(cut(v[0])) | t.s[0], asu(cut(v[1])) | t.s[1], asu(cut(v[2])) | t.s[2], asu(cut(v[3])) | t.s[3]};
}
__device__ __forceinline__ float uval(unsigned u) { return asf(u & ~1023u); }
__device__ __forceinline__ bool unit_bad(const v4u& v, const Tag& t) {
  return ((((v[0] ^ t.s[0]) | (v[1] ^ t.s[1])) | ((v[2] ^ t.s[2]) | (v[3] ^ t.s[3]))) & 1023u) != 0u;
}
__device__ __forceinline__ void st16(rsrc_t rs, int off, v4u v) { __builtin_amdgcn_raw_buffer_store_b128(v, rs, off, 0, AUX_SC1); }
__device__ __forceinline__ v4u ld16(rsrc_t rs, int off) { return __builtin_amdgcn_raw_buffer_load_b128(rs, off, 0, AUX_SC1); }
template <int N, int SLEEP>
__device__ __forceinline__ void settle(rsrc_t rs, const int (&off)[N], v4u (&v)[N], const Tag& t) {
  for (unsigned sp = 0; sp < SPIN_CAP; ++sp) {
    bool bad[N], any = false;
#pragma unroll
    for (int k = 0; k < N; ++k) { bad[k] = unit_bad(v[k], t); any |= bad[k]; }
    if (__builtin_amdgcn_ballot_w64(any) == 0ull) break;
    __builtin_amdgcn_s_sleep(SLEEP);
    asm volatile("" ::: "memory");
#pragma unroll
    for (int k = 0; k < N; ++k)
      if (bad[k]) v[k] = ld16(rs, off[k]);
  }
}
template <int SLEEP>
__device__ __forceinline__ void lds_wait(volatile LAS unsigned* f, unsigned want) {
#pragma clang loop unroll(disable)
  for (unsigned sp = 0; sp < (1u << 24); ++sp) {
    if ((unsigned)__builtin_amdgcn_readfirstlane(*f) >= want) break;
    __builtin_amdgcn_s_sleep(SLEEP);
  }
  asm volatile("" ::: "memory");
}
__device__ __forceinline__ void pace(unsigned tz, int ticks) {
#pragma clang loop unroll(disable)
  for (int sp = 0; sp < 4096; ++sp) {
    if ((int)((unsigned)__builtin_amdgcn_s_memrealtime() - tz) >= ticks) break;
    __builtin_amdgcn_s_sleep(1);
  }
  PIN();
}
__device__ __forceinline__ void lds_inc(volatile LAS unsigned* f, int lane) {
  LGKM0();
  if (lane == 0) __hip_atomic_fetch_add((LAS unsigned*)f, 1u, __ATOMIC_RELAXED, __HIP_MEMORY_SCOPE_WORKGROUP);
}
__device__ __forceinline__ f32x4 fold8(const f32x4 (&acc)[8], bool rs) {
  f32x4 r;
#pragma unroll
  for (int i = 0; i < 4; ++i) {
    const float a0 = opq(acc[i][0]), a1 = opq(acc[i][1]), b0 = opq(acc[4 + i][0]), b1 = opq(acc[4 + i][1]);
    r[i] = (rs ? b1 : a0) + dpp<ROR8>(rs ? a1 : b0);
  }
  return r;
}
#define ST(i) do {} while (0)
#define STDUMP() do {} while (0)
}

__global__ void __launch_bounds__(NTHR, 2)
fused_decode(const u16*, const u16*, const u16*, const u16*, const u16*, const u16*, const u16*, const u16*, u16*, unsigned char*) {
  v16u ka;
  v4u kb;
  asm volatile("s_load_dwordx16 %0, %2, 0x0\n\ts_load_dwordx4 %1, %2, 0x40" : "=&s"(ka), "=&s"(kb) : "s"(__builtin_amdgcn_kernarg_segment_ptr()));
  __shared__ __attribute__((aligned(16))) unsigned char smem[L_BYTES];
  LAS unsigned char* L = (LAS unsigned char*)smem;
  const int tid = threadIdx.x, lane = tid & 63;
  const int wave = __builtin_amdgcn_readfirstlane(tid >> 6), w4 = wave & 3;
  const int bid = blockIdx.x, xcd = bid & 7, idx = bid >> 3;
  const int g = lane >> 4, c = lane & 15;
  ST(0);
  const unsigned long long tstart = __builtin_amdgcn_s_memrealtime();
  const bool isV = wave < 4;
  volatile LAS unsigned* flag = (volatile LAS unsigned*)(L + L_FLAG);
  if (tid < 16) flag[tid] = 0u;
  if (isV) __builtin_amdgcn_s_setprio(3); else __builtin_amdgcn_s_setprio(2);
  const int b = idx >> 3, split = idx & 7;
  const int sbase = ((b * 8 + xcd) * 1024 + 256 * w4 + split) * 1024;
  const bool rs = (c & 8) != 0;
  const int koff = ((c >> 1) & 1) * 8192 + (c >> 2) * 256 + (2 * g + (c & 1)) * 16;
  const int tile = idx < 16 ? ((xcd + 4) & 7) * 16 + idx : 128 + (idx - 16) * 8 + xcd;
  const bool has_tile = tile < 240;
  v4u R[12];
#define ARGP(T, v, i) ((T)(((unsigned long long)(v)[2 * (i) + 1] << 32) | (v)[2 * (i)]))
  const int cgp = has_tile ? tile >> 1 : 0, k0 = (tile & 1) * 256 + w4 * 64;
  const int ldb = cgp < 64 ? 8192 : 3584;
  const int sb = k0 * ldb + (cgp < 64 ? cgp : cgp < 92 ? cgp - 64 : cgp - 92) * 128;
  const int va = (c >> 2) * 1024 + (c & 3) * 64 + g * 16, vb = ((c >> 3) * 32 + g * 8) * ldb + (c & 7) * 16;
  const int xo = (k0 >> 7) * 256;
  const unsigned wbytes = has_tile ? 512u * ldb : 0u;
  asm volatile("" ::"s"(sb), "s"(ldb), "s"(xo), "s"(wbytes), "s"(sbase), "v"(va), "v"(vb), "v"(koff));
  PIN();
  asm volatile("s_waitcnt lgkmcnt(0)" : "+s"(ka), "+s"(kb)::"memory");
  const u16 *x = ARGP(const u16*, ka, 0), *kc = ARGP(const u16*, ka, 1), *vc = ARGP(const u16*, ka, 2), *wo = ARGP(const u16*, ka, 4), *w2 = ARGP(const u16*, ka, 7);
  u16* out = ARGP(u16*, kb, 0);
  unsigned char* ws = ARGP(unsigned char*, kb, 1);
  unsigned epoch;
  asm volatile("s_load_dword %0, %1, 0x0" : "=&s"(epoch) : "s"(ws));
  unsigned long long tk;
  asm volatile("s_memrealtime %0" : "=&s"(tk));
  const unsigned jit = (((unsigned)(bid * 4 + w4) * 0x9E3779B1u) >> 16) * JIT >> 16;
  const rsrc_t rk = mkrsrc(kc, 4u * 8 * 4096 * 128 * 2);
  if (isV) {
    const u16 *wq = ARGP(const u16*, ka, 3), *w1 = ARGP(const u16*, ka, 5), *w3 = ARGP(const u16*, ka, 6);
    asm("" : "+s"(wq)); asm("" : "+s"(w1)); asm("" : "+s"(w3));
    const u16* wmat = cgp < 64 ? wq : cgp < 92 ? w1 : w3;
    const rsrc_t rx = mkrsrc(x, 4096u), rw = mkrsrc(wmat, wbytes);
#pragma unroll
    for (int j = 0; j < WFIRST; ++j) R[4 + j] = __builtin_amdgcn_raw_buffer_load_b128(rw, vb, sb + j * ldb, AUX_NT);
    PIN();
#pragma unroll
    for (int j = 0; j < 4; ++j) R[j] = __builtin_amdgcn_raw_buffer_load_b128(rx, va, (xo + j * 256) & 768, 0);
    PIN();
#pragma unroll
    for (int j = WFIRST; j < 8; ++j) R[4 + j] = __builtin_amdgcn_raw_buffer_load_b128(rw, vb, sb + j * ldb, AUX_NT);
  } else {
#pragma unroll
    for (int j = 0; j < KPRE; ++j) R[j] = __builtin_amdgcn_raw_buffer_load_b128(rk, koff, sbase + (j >> 1) * 16384 + (j & 1) * 128, AUX_NT);
  }
  PIN();
  __builtin_amdgcn_s_barrier();
  asm volatile("" ::: "memory");
  PIN();
  unsigned tag;
  unsigned long long tk2;
  asm volatile("s_waitcnt lgkmcnt(0)\n\ts_add_u32 %0, %2, 1\n\ts_mov_b64 %1, %3" : "=&s"(tag), "=&s"(tk2) : "s"(epoch), "s"(tk) : "memory", "scc");
  unsigned tz = (unsigned)tk2 + CAP;
  if (isV) VMCNT(0); else VMCNT(KPRE - 1);
  const unsigned ta = (unsigned)__builtin_amdgcn_s_memrealtime();
  if (isV && lane == 0) __hip_atomic_fetch_max((LAS unsigned*)(flag + 9), ta, __ATOMIC_RELAXED, __HIP_MEMORY_SCOPE_WORKGROUP);
  PIN();
  const Tag T = mktag(tag);
  const rsrc_t rws = mkrsrc(ws, WS_BYTES);

  if (isV) {
    {
      float s = 0.f;
#pragma unroll
      for (int i = 0; i < 4; ++i)
#pragma unroll
        for (int k = 0; k < 4; ++k) s += lo16(R[i][k]) + hi16(R[i][k]);
      s += dpp<XOR1>(s); s += dpp<XOR2>(s);
      const float mean = xrow_sum(s) * (1.0f / 512.0f);
      float q = 0.f;
#pragma unroll
      for (int i = 0; i < 4; ++i)
#pragma unroll
        for (int k = 0; k < 4; ++k) {
          const float d0 = lo16(R[i][k]) - mean, d1 = hi16(R[i][k]) - mean;
          q = fmaf(d0, d0, q); q = fmaf(d1, d1, q);
        }
      q += dpp<XOR1>(q); q += dpp<XOR2>(q);
      const float var = rbf(xrow_sum(q) * (1.0f / 512.0f));
      const float den = rbf(__builtin_amdgcn_sqrtf(rbf(var + 1e-5f)));
      const float inv = __builtin_amdgcn_rcpf(den), mb = rbf(mean);
      v4u A;
#pragma unroll
      for (int k = 0; k < 4; ++k) {
        const unsigned d = pk2(lo16(R[0][k]) - mb, hi16(R[0][k]) - mb);
        const unsigned a = pk2(lo16(d) * inv, hi16(d) * inv), a2 = dppu<0xEE>(a);
        A[k] = (w4 & 1) ? a2 : a;
      }
      ST(1);
      f32x4 acc[8];
      {
        v4u bf[8];
#pragma unroll
        for (int t = 0; t < 4; ++t) {
#pragma unroll
          for (int i = 0; i < 8; ++i) {
            bf[i][t] = permsel(R[4 + 2 * t], R[5 + 2 * t], i);
            if (t == 3) acc[i] = mfma(A, bf[i], f32x4{0.f, 0.f, 0.f, 0.f});
          }
          PIN();
        }
      }
      *(LAS f32x4*)(L + L_PX + w4 * 1024 + lane * 16) = fold8(acc, rs);
      LGKM0();
      unsigned old = 0u;
      if (lane == 0) old = __hip_atomic_fetch_add((LAS unsigned*)(flag + 1), 1u, __ATOMIC_RELAXED, __HIP_MEMORY_SCOPE_WORKGROUP);
      if (__builtin_amdgcn_readfirstlane(old) == 3u && has_tile) {
        asm volatile("" ::: "memory");
        const f32x4 o = (*(LAS f32x4*)(L + L_PX + lane * 16) + *(LAS f32x4*)(L + L_PX + 1024 + lane * 16)) +
                        (*(LAS f32x4*)(L + L_PX + 2048 + lane * 16) + *(LAS f32x4*)(L + L_PX + 3072 + lane * 16));
        st16(rws, WS_PG + tile * 1024 + lane * 16, unit(o, T));
        if ((idx & 15) == 5 && lane == 0)
          __builtin_amdgcn_raw_buffer_store_b64(v2u{flag[9], tag}, rws, WS_CK + ((idx >> 4) * 8 + xcd) * 128, 0, AUX_SC1);
      }
      PIN();
      ST(2);
    }
    __builtin_amdgcn_s_setprio(0);
    if (w4 == 0) {
      pace(tz, CKT);
      const v2u ck = __builtin_amdgcn_raw_buffer_load_b64(rws, c * 128, WS_CK, AUX_SC1);
      const int dt = (int)(ck[0] - tz);
      const bool ok = ck[1] == tag && dt > -400 && dt < 400;
      float sd = ok ? (float)dt : 0.f;
      sd += dpp<XOR1>(sd); sd += dpp<XOR2>(sd); sd += dpp<ROR4>(sd); sd += dpp<ROR8>(sd);
      const int n = __builtin_popcount((unsigned)__builtin_amdgcn_ballot_w64(ok) & 0xffffu);
      const int mean = __builtin_amdgcn_readfirstlane((int)(sd * __builtin_amdgcn_rcpf((float)(n > 0 ? n : 1))));
      if (lane == 0) flag[8] = (tz + (n > 0 ? mean + GOFF : 0)) | 1u;
    }
    lds_wait<4>(flag + 8, 1u);
    tz = __builtin_amdgcn_readfirstlane(flag[8]) - jit;
    const rsrc_t rv = mkrsrc(vc, 4u * 8 * 4096 * 128 * 2);
    const int voff = lane * 16;
    v4u V[32];
    v4u bf[4][8];
#define VPERM(pr) do { _Pragma("unroll") for (int i = 0; i < 8; ++i) bf[(pr) >> 2][i][(pr) & 3] = permsel(V[2 * (pr)], V[2 * (pr) + 1], i); } while (0)
#pragma unroll
    for (int j = 0; j < 32; ++j) {
      if (j >= VD2) VMCNT(VD2 - 1);
      if ((j & 1) == 0) pace(tz, PT(32 + j));
      V[j] = __builtin_amdgcn_raw_buffer_load_b128(rv, voff, sbase + ((j & 24) + 4 * (j & 1) + ((j >> 1) & 3)) * 8192, AUX_NT);
      PIN();
      if (j >= VD2 + 1 && ((j - VD2) & 1)) { VPERM((j - VD2) >> 1); PIN(); }
    }
    lds_inc(flag + 4, lane);
    ST(3);
    lds_wait<1>(flag + 3, 4u);
    v4u P[4];
#pragma unroll
    for (int G = 0; G < 4; ++G) P[G] = *(LAS v4u*)(L + L_P + w4 * 1024 + G * 256 + (lane & ~3) * 4);
    ST(4);
    f32x4 acc[8];
#pragma unroll
    for (int i = 0; i < 8; ++i) acc[i] = f32x4{0.f, 0.f, 0.f, 0.f};
    constexpr int DONE = (32 - VD2) >> 1;
#pragma unroll
    for (int G = 0; G < 4; ++G) {
#pragma unroll
      for (int t = 0; t < 4; ++t)
        if (4 * G + t >= DONE) { VPERM(4 * G + t); if (t < 3) PIN(); }
#pragma unroll
      for (int i = 0; i < 8; ++i) acc[i] = mfma(P[G], bf[G][i], acc[i]);
      PIN();
    }
    *(LAS f32x4*)(L + L_ORED + w4 * 2048 + lane * 16) = f32x4{acc[0][0], acc[1][0], acc[2][0], acc[3][0]};
    *(LAS f32x4*)(L + L_ORED + w4 * 2048 + 1024 + lane * 16) = f32x4{acc[4][0], acc[5][0], acc[6][0], acc[7][0]};
    ST(5);
    wg_barrier();
    ST(6);
    if (lane < 32) {
      const int ro = (lane & 1) * 1024 + (w4 * 16 + (lane >> 1)) * 16;
      const f32x4 o = (*(LAS f32x4*)(L + L_ORED + ro) + *(LAS f32x4*)(L + L_ORED + 2048 + ro)) +
                      (*(LAS f32x4*)(L + L_ORED + 4096 + ro) + *(LAS f32x4*)(L + L_ORED + 6144 + ro));
      st16(rws, WS_OG + ((b * 8 + xcd) * 4 + w4) * 4096 + g * 2048 + split * 256 + c * 16, unit(o, T));
    }
    ST(7);
    wg_barrier();
    STDUMP();
    return;
  }

  v4u K[32];
#pragma unroll
  for (int j = 0; j < KPRE; ++j) K[j] = R[j];
  if ((int)(ta - tz) < 0) tz = ta;
  PIN();
#pragma unroll
  for (int j = KPRE; j < 32; ++j) {
    if ((j & 1) == 0 && PT(j) > 0) pace(tz, PT(j));
    K[j] = __builtin_amdgcn_raw_buffer_load_b128(rk, koff, sbase + (j >> 1) * 16384 + (j & 1) * 128, AUX_NT);
    PIN();
  }
  {
    ST(1);
    {
      const int off[1] = {WS_PG + (xcd * 16 + 4 * w4 + g) * 1024 + b * 256 + c * 16};
      v4u gv[1] = {ld16(rws, off[0])};
      settle<1, 8>(rws, off, gv, T);
      const float q0 = fold16(uval(gv[0][0]), uval(gv[0][2])), q1 = fold16(uval(gv[0][1]), uval(gv[0][3]));
      *(LAS unsigned*)(L + L_QS + (128 * w4 + 64 * (g >> 1) + 8 * (c & 7) + 4 * (c >> 3) + 2 * (g & 1)) * 2) = pk2(q0, q1);
      lds_inc(flag, lane);
      lds_wait<1>(flag, 4u);
    }
    ST(2);
    v4u qf[2][2];
    {
      const bool a1 = (c & 2) != 0;
#pragma unroll
      for (int p = 0; p < 2; ++p) {
        const v4u t = *(LAS v4u*)(L + L_QS + (c >> 2) * 256 + (8 * p + 2 * g + (c & 1)) * 16);
#pragma unroll
        for (int k = 0; k < 4; ++k) { qf[p][0][k] = a1 ? 0u : t[k]; qf[p][1][k] = a1 ? t[k] : 0u; }
      }
    }
    float s[8];
    {
      f32x4 d[8];
#pragma unroll
      for (int n = 0; n < 8; ++n) d[n] = f32x4{0.f, 0.f, 0.f, 0.f};
#pragma unroll
      for (int z = 0; z < 4; ++z) {
#pragma unroll
        for (int n = 0; n < 8; ++n) d[n] = mfma(K[4 * n + z], qf[z & 1][z >> 1], d[n]);
        PIN();
      }
      const bool u1 = (c & 1) != 0;
#pragma unroll
      for (int n = 0; n < 8; ++n) {
        const float d0 = opq(d[n][0]), d1 = opq(d[n][1]), d2 = opq(d[n][2]), d3 = opq(d[n][3]);
        s[n] = (u1 ? d3 : d0) + dpp<XOR1>(u1 ? d1 : d2);
      }
    }
    float m = -INFINITY;
#pragma unroll
    for (int n = 0; n < 8; n += 2) {
      const unsigned r1 = pk2(s[n], s[n + 1]);
      const unsigned r2 = pk2(lo16(r1) * SCALE, hi16(r1) * SCALE);
      s[n] = lo16(r2); s[n + 1] = hi16(r2);
      m = fmaxf(m, fmaxf(s[n], s[n + 1]));
    }
    m = fmaxf(m, dpp<XOR1>(m)); m = fmaxf(m, dpp<XOR2>(m));
    m = xrow_max(m) * LOG2E;
    if (g == 0 && (c & 3) == 0) *(LAS float*)(L + L_MX + ((c >> 2) * 4 + w4) * 4) = m;
    lds_inc(flag + 2, lane);
    lds_wait<1>(flag + 2, 4u);
    {
      const f32x4 ma = *(LAS f32x4*)(L + L_MX + (c >> 2) * 16);
      const float m2 = cut(fmaxf(fmaxf(ma[0], ma[1]), fmaxf(ma[2], ma[3])));
      float l = 0.f;
#pragma unroll
      for (int G = 0; G < 4; ++G) {
        const float p0 = __builtin_amdgcn_exp2f(fmaf(s[2 * G], LOG2E, -m2)), p1 = __builtin_amdgcn_exp2f(fmaf(s[2 * G + 1], LOG2E, -m2));
        l += p0 + p1;
        *(LAS unsigned*)(L + L_P + w4 * 1024 + G * 256 + lane * 4) = pk2(p0, p1);
      }
      l += dpp<XOR1>(l); l += dpp<XOR2>(l);
      l = xrow_sum(l);
      if (g == 0 && (c & 3) == 0) *(LAS float*)(L + L_LS + ((c >> 2) * 4 + w4) * 4) = l;
      lds_inc(flag + 3, lane);
    }
    ST(3);
  }
  const int kvc = (2 * (xcd & 3) + 2 + 4 * (xcd >> 2) + (idx >> 4)) & 7, r8 = (idx >> 1) & 7, cg = 2 * (xcd >> 2) + (idx & 1);
  const int cgf = 2 * (1 - (xcd >> 2)) + (xcd & 1), bf = 2 * ((xcd >> 1) & 1) + (idx >> 4), fa = idx & 15;
  const int rg = kvc * 8 + r8;
  const int fc = 2 * (fa & 7) + (lane & 1);
  const int col = 128 * cgf + 64 * (fa >> 3) + 8 * (fc & 7) + 4 * (fc >> 3) + 2 * (g >> 1) + (g & 1);
  v4u W[12];
  unsigned xr = 0u;
  if (w4 == 0) {
    xr = __builtin_amdgcn_raw_buffer_load_b16(mkrsrc(x, 4096u), (bf * 512 + col) * 2, 0, 0);
    lds_wait<1>(flag + 3, 4u);
    if (lane < 4) {
      const f32x4 ma = *(LAS f32x4*)(L + L_MX + lane * 16), la = *(LAS f32x4*)(L + L_LS + lane * 16);
      st16(rws, WS_ML + ((b * 8 + xcd) * 4 + lane) * 128 + split * 16,
           unit(f32x4{fmaxf(fmaxf(ma[0], ma[1]), fmaxf(ma[2], ma[3])), (la[0] + la[1]) + (la[2] + la[3]), 0.f, 0.f}, T));
    }
  } else if (w4 == 1) {
    const int qd = c & 7, f = 28 * rg + 4 * (qd < 7 ? qd : 6);
    const int vo = WS_PG + (128 + 2 * (f >> 6) + (c >> 3)) * 1024 + g * 256 + (((f >> 2) & 1) * 8 + ((f & 63) >> 3)) * 16;
    const int off[2] = {vo, vo + 56 * 1024};
    v4u gv[2] = {ld16(rws, off[0]), ld16(rws, off[1])};
    settle<2, 16>(rws, off, gv, T);
    float hv[4];
#pragma unroll
    for (int k = 0; k < 4; ++k) {
      const float g0 = uval(gv[0][k]), u0 = uval(gv[1][k]);
      const float gt = rbf(g0 + dpp<ROR8>(g0)), up = rbf(u0 + dpp<ROR8>(u0));
      const float sl = rbf(gt * __builtin_amdgcn_rcpf(1.0f + __builtin_amdgcn_exp2f(-gt * LOG2E)));
      hv[k] = sl * up;
    }
    if (c < 8) *(LAS v2u*)(L + L_HA + g * 64 + c * 8) = qd < 7 ? v2u{pk2(hv[0], hv[1]), pk2(hv[2], hv[3])} : v2u{0u, 0u};
  }
  float w0, w1_;
  {
    lds_wait<4>(flag + 8, 1u);
    tz = __builtin_amdgcn_readfirstlane(flag[8]) - jit;
    pace(tz, PEND - MLT);
    const int mloff[1] = {WS_ML + ((w4 * 8 + kvc) * 4 + (r8 >> 1)) * 128 + (4 * (c & 1) + g) * 16};
    v4u ml[1] = {ld16(rws, mloff[0])};
    settle<1, 4>(rws, mloff, ml, T);
    const float ms = uval(ml[0][0]);
    const float M = xrow_max(fmaxf(ms, dpp<XOR1>(ms)));
    float wa = __builtin_amdgcn_exp2f(ms - M);
    float Ls = wa * uval(ml[0][1]);
    Ls = xrow_sum(Ls + dpp<XOR1>(Ls));
    wa *= __builtin_amdgcn_rcpf(Ls);
    w0 = dpp<0xA0>(wa); w1_ = dpp<0xF5>(wa);
    asm volatile("" : "+v"(w0), "+v"(w1_));
  }
  if (w4 >= 2) {
    __builtin_amdgcn_s_setprio(0);
    const rsrc_t rwo = mkrsrc(wo, 4096u * 512 * 2), rw2 = mkrsrc(w2, 1792u * 512 * 2);
    const int colb = (128 * cg + 64 * (w4 - 2)) * 2;
    const int vo = ((c >> 3) * 32 + g * 8) * 1024 + (c & 7) * 16;
    const int vo2 = (rs && g == 3) ? 0x7ff00000 : ((c >> 3) * 16 + g * 4) * 1024 + (c & 7) * 16;
    asm volatile("" ::"v"(vo), "v"(vo2));
    lds_wait<2>(flag + 4, 4u);
    pace(tz, PEND + WOT);
#pragma unroll
    for (int j = 0; j < 4; ++j) W[8 + j] = __builtin_amdgcn_raw_buffer_load_b128(rw2, vo2, (28 * rg + j) * 1024 + colb, AUX_NT);
#pragma unroll
    for (int j = 0; j < 8; ++j) W[j] = __builtin_amdgcn_raw_buffer_load_b128(rwo, vo, (64 * rg + j) * 1024 + colb, AUX_NT);
    PIN();
  }
  v4u Bo[8];
  f32x4 acc[8];
  ST(5);
  wg_barrier();
  wg_barrier();
  {
    __builtin_amdgcn_s_sleep(PDELAY1);
    const int so = WS_OG + ((w4 * 8 + kvc) * 8 + r8) * 2048 + lane * 16;
    const int off[2] = {so, so + 1024};
    v4u gv[2] = {ld16(rws, off[0]), ld16(rws, off[1])};
    PIN();
    if (w4 >= 2) {
      const v2u ha = *(LAS v2u*)(L + L_HA + (c >> 2) * 64 + (c & 1) * 32 + g * 8);
#pragma unroll
      for (int i = 0; i < 8; ++i)
        acc[i] = mfma(v4u{ha[0], ha[1], 0u, 0u}, v4u{permsel(W[8], W[9], i), permsel(W[10], W[11], i), 0u, 0u}, f32x4{0.f, 0.f, 0.f, 0.f});
      PIN();
#pragma unroll
      for (int t = 0; t < 4; ++t) {
#pragma unroll
        for (int i = 0; i < 8; ++i) Bo[i][t] = permsel(W[2 * t], W[2 * t + 1], i);
        PIN();
      }
    }
    PIN();
    settle<2, POLL_SLEEP>(rws, off, gv, T);
    float o[4];
#pragma unroll
    for (int k = 0; k < 4; ++k) o[k] = w0 * uval(gv[0][k]) + w1_ * uval(gv[1][k]);
    const float r = fold16(fold32(o[0], o[2]), fold32(o[1], o[3]));
    *(LAS u16*)(L + L_OA + w4 * 128 + (4 * c + g) * 2) = (u16)pk2(r, r);
  }
  ST(6);
  wg_barrier();
  if (w4 >= 2) {
    const v4u a0 = *(LAS v4u*)(L + L_OA + (c >> 2) * 128 + (c & 1) * 64 + g * 16);
#pragma unroll
    for (int i = 0; i < 8; ++i) acc[i] = mfma(a0, Bo[i], acc[i]);
    st16(rws, WS_YG + ((cg * 64 + rg) * 2 + (w4 - 2)) * 1024 + lane * 16, unit(fold8(acc, rs), T));
    wg_barrier();
  } else if (w4 == 0) {
    wg_barrier();
    __builtin_amdgcn_s_sleep(PDELAY);
    const int vy = WS_YG + (cgf * 64 + (lane >> 1)) * 2048 + (fa >> 3) * 1024 + bf * 256 + fc * 16;
    const int off[2] = {vy, vy + 32 * 2048};
    v4u y[2] = {ld16(rws, off[0]), ld16(rws, off[1])};
    settle<2, POLL2_SLEEP>(rws, off, y, T);
    float o[4];
#pragma unroll
    for (int k = 0; k < 4; ++k) o[k] = uval(y[0][k]) + uval(y[1][k]);
    float r = fold16(fold32(o[0], o[2]), fold32(o[1], o[3]));
    r += dpp<XOR2>(r); r += dpp<ROR4>(r); r += dpp<ROR8>(r);
    r += lo16(xr);
    if (c < 2) __builtin_amdgcn_raw_buffer_store_b16((u16)pk2(r, r), mkrsrc(out, 4096u), (bf * 512 + col) * 2, 0, 0);
    if (bid == 0 && lane == 0) __builtin_amdgcn_raw_buffer_store_b32(tag, rws, 0, WS_E, AUX_SC1);
    if (bid == 0) { const unsigned long long lim = 4000; for (int i = 0; i < 1000000; ++i) { if (__builtin_amdgcn_s_memrealtime() - tstart >= lim) break; __builtin_amdgcn_s_sleep(1); } }
  } else if (STAMP) {
    wg_barrier();
  }
  ST(7);
  STDUMP();
}

extern "C" void kernel_launch(void* const* d_in, const int* in_sizes, int n_in, void* d_out, int out_size, void* d_ws, size_t ws_size,
                              hipStream_t stream) {
  (void)in_sizes; (void)n_in; (void)out_size; (void)ws_size;
  fused_decode<<<256, NTHR, 0, stream>>>((const u16*)d_in[0], (const u16*)d_in[1], (const u16*)d_in[2], (const u16*)d_in[3], (const u16*)d_in[4],
                                         (const u16*)d_in[6], (const u16*)d_in[7], (const u16*)d_in[8], (u16*)d_out, (unsigned char*)d_ws);
}
```

```cpp
#include <hip/hip_runtime.h>
#include <stdint.h>

typedef unsigned short u16;
typedef unsigned v4u __attribute__((ext_vector_type(4)));
typedef unsigned v2u __attribute__((ext_vector_type(2)));
typedef unsigned v16u __attribute__((ext_vector_type(16)));
typedef __bf16 bf16x8 __attribute__((ext_vector_type(8)));
typedef __bf16 bf16x2 __attribute__((ext_vector_type(2)));
typedef float f32x4 __attribute__((ext_vector_type(4)));
typedef float f32x2 __attribute__((ext_vector_type(2)));
#define LAS __attribute__((address_space(3)))

#define STAMP 0
#define VD2 16
#define KPRE 4
#define PEND 1025
#define PD 145
#define PT(r) (PEND - ((63 - (r)) * PD) / 10)
#define WOT 45
#define POLL_SLEEP 1
#define POLL2_SLEEP 1
#define PDELAY1 12
#define PDELAY 8
#define JIT 60
#define OAUX 16
#define T1 0
#define T2 0
#define CODE_BYTES 15232
#define PFMASK 3
#define WOE 0
#define WOT0 -30
#define CKJ 0
#define MLT 80
#define WFIRST 0
#define CKT 320
#define GOFF -5
#define MAPPING 1

namespace {
constexpr int AUX_NT = 2, AUX_SC1 = 16, RSRC_W3 = 0x00020000;
constexpr int NTHR = 512;
constexpr unsigned SPIN_CAP = 1u << 16;
constexpr int WS_E = 0;
constexpr int WS_PG = 4096;
constexpr int WS_ML = WS_PG + 240 * 1024;
constexpr int WS_CK = WS_ML + 128 * 128;
constexpr int WS_OG = WS_CK + 16 * 128;
constexpr int WS_YG = WS_OG + 128 * 4096;
constexpr int WS_ST = WS_YG + 4 * 64 * 2048;
constexpr int WS_BYTES = WS_ST + 256 * 8 * 8 * 8;
constexpr int L_ORED = 0;
constexpr int L_PX = L_ORED + 8192;
constexpr int L_P = L_PX + 4096;
constexpr int L_QS = L_P + 4096;
constexpr int L_MX = L_QS + 1024;
constexpr int L_LS = L_MX + 64;
constexpr int L_OA = L_LS + 64;
constexpr int L_HA = L_OA + 512;
constexpr int L_FLAG = L_HA + 256;
constexpr int L_BYTES = L_FLAG + 64;

constexpr float LOG2E = 1.4426950408889634f;
constexpr float SCALE = 0.08838834764831845f;

__device__ __forceinline__ float asf(unsigned u) { return __builtin_bit_cast(float, u); }
__device__ __forceinline__ unsigned asu(float f) { return __builtin_bit_cast(unsigned, f); }
__device__ __forceinline__ unsigned pk2(float lo, float hi) {
  f32x2 p = {lo, hi};
  return __builtin_bit_cast(unsigned, __builtin_convertvector(p, bf16x2));
}
__device__ __forceinline__ float lo16(unsigned u) { return asf(u << 16); }
__device__ __forceinline__ float hi16(unsigned u) { return asf(u & 0xffff0000u); }
__device__ __forceinline__ float rbf(float f) { return lo16(pk2(f, f)); }
template <int CTRL> __device__ __forceinline__ unsigned dppu(unsigned x) { return (unsigned)__builtin_amdgcn_mov_dpp((int)x, CTRL, 0xf, 0xf, true); }
template <int CTRL> __device__ __forceinline__ float dpp(float x) { return asf(dppu<CTRL>(asu(x))); }
constexpr int XOR1 = 0xB1, XOR2 = 0x4E, ROR8 = 0x128, ROR4 = 0x124;
__device__ __forceinline__ float opq(float x) { asm("" : "+v"(x)); return x; }
__device__ __forceinline__ float fold32(float a, float b) {
  auto t = __builtin_amdgcn_permlane32_swap(asu(a), asu(b), false, false);
  return asf(t[0]) + asf(t[1]);
}
__device__ __forceinline__ float fold16(float a, float b) {
  auto t = __builtin_amdgcn_permlane16_swap(asu(a), asu(b), false, false);
  return asf(t[0]) + asf(t[1]);
}
__device__ __forceinline__ float xrow_sum(float x) { x = fold16(x, x); return fold32(x, x); }
__device__ __forceinline__ float xrow_max(float x) {
  auto s = __builtin_amdgcn_permlane16_swap(asu(x), asu(x), false, false);
  x = fmaxf(asf(s[0]), asf(s[1]));
  auto t = __builtin_amdgcn_permlane32_swap(asu(x), asu(x), false, false);
  return fmaxf(asf(t[0]), asf(t[1]));
}
__device__ __forceinline__ f32x4 mfma(v4u a, v4u b, f32x4 c) {
  return __builtin_amdgcn_mfma_f32_16x16x32_bf16(__builtin_bit_cast(bf16x8, a), __builtin_bit_cast(bf16x8, b), c, 0, 0, 0);
}
__device__ __forceinline__ unsigned permsel(const v4u& e, const v4u& o, int i) {
  return __builtin_amdgcn_perm(o[i >> 1], e[i >> 1], (i & 1) ? 0x07060302u : 0x05040100u);
}
#define PIN() __builtin_amdgcn_sched_barrier(0)
#define LGKM0() asm volatile("s_waitcnt lgkmcnt(0)" ::: "memory")
#define VMCNT(n) asm volatile("s_waitcnt vmcnt(%0)" ::"i"(n) : "memory")
__device__ __forceinline__ void wg_barrier() {
  PIN();
  LGKM0();
  __builtin_amdgcn_s_barrier();
  asm volatile("" ::: "memory");
  PIN();
}
typedef __amdgpu_buffer_rsrc_t rsrc_t;
__device__ __forceinline__ rsrc_t mkrsrc(const void* p, unsigned bytes) { return __builtin_amdgcn_make_buffer_rsrc((void*)p, 0, (int)bytes, RSRC_W3); }
struct Tag { unsigned s[4]; };
__device__ __forceinline__ Tag mktag(unsigned t) {
  return Tag{{t & 1023u, (t + (t >> 10) + 341u) & 1023u, (t + (t >> 20) + 682u) & 1023u, (t + (t >> 30) + 171u) & 1023u}};
}
__device__ __forceinline__ float cut(float a) { return asf((asu(a) + 512u) & ~1023u); }
__device__ __forceinline__ v4u unit(f32x4 v, const Tag& t) {
  return v4u{asu(cut(v[0])) | t.s[0], asu(cut(v[1])) | t.s[1], asu(cut(v[2])) | t.s[2], asu(cut(v[3])) | t.s[3]};
}
__device__ __forceinline__ float uval(unsigned u) { return asf(u & ~1023u); }
__device__ __forceinline__ bool unit_bad(const v4u& v, const Tag& t) {
  return ((((v[0] ^ t.s[0]) | (v[1] ^ t.s[1])) | ((v[2] ^ t.s[2]) | (v[3] ^ t.s[3]))) & 1023u) != 0u;
}
__device__ __forceinline__ void st16(rsrc_t rs, int off, v4u v) { __builtin_amdgcn_raw_buffer_store_b128(v, rs, off, 0, AUX_SC1); }
__device__ __forceinline__ v4u ld16(rsrc_t rs, int off) { return __builtin_amdgcn_raw_buffer_load_b128(rs, off, 0, AUX_SC1); }
template <int N, int SLEEP>
__device__ __forceinline__ void settle(rsrc_t rs, const int (&off)[N], v4u (&v)[N], const Tag& t) {
  for (unsigned sp = 0; sp < SPIN_CAP; ++sp) {
    bool bad[N], any = false;
#pragma unroll
    for (int k = 0; k < N; ++k) { bad[k] = unit_bad(v[k], t); any |= bad[k]; }
    if (__builtin_amdgcn_ballot_w64(any) == 0ull) break;
    __builtin_amdgcn_s_sleep(SLEEP);
    asm volatile("" ::: "memory");
#pragma unroll
    for (int k = 0; k < N; ++k)
      if (bad[k]) v[k] = ld16(rs, off[k]);
  }
}
template <int SLEEP>
__device__ __forceinline__ void lds_wait(volatile LAS unsigned* f, unsigned want) {
#pragma clang loop unroll(disable)
  for (unsigned sp = 0; sp < (1u << 24); ++sp) {
    if ((unsigned)__builtin_amdgcn_readfirstlane(*f) >= want) break;
    __builtin_amdgcn_s_sleep(SLEEP);
  }
  asm volatile("" ::: "memory");
}
__device__ __forceinline__ void pace(unsigned tz, int ticks) {
#pragma clang loop unroll(disable)
  for (int sp = 0; sp < 4096; ++sp) {
    if ((int)((unsigned)__builtin_amdgcn_s_memrealtime() - tz) >= ticks) break;
    __builtin_amdgcn_s_sleep(1);
  }
  PIN();
}
__device__ __forceinline__ int mean_offset(v2u ck, unsigned mine, unsigned tag) {
  int dt = (int)(ck[0] - mine);
  const bool ok = ck[1] == tag && dt > -300 && dt < 300;
  dt = ok ? dt : 0;
  dt += (int)dppu<XOR1>(dt); dt += (int)dppu<XOR2>(dt); dt += (int)dppu<ROR4>(dt);
  const int n = __builtin_popcount((unsigned)__builtin_amdgcn_ballot_w64(ok) & 0xffu);
  return (int)((float)dt * __builtin_amdgcn_rcpf((float)(n > 0 ? n : 1)));
}
__device__ __forceinline__ void lds_inc(volatile LAS unsigned* f, int lane) {
  LGKM0();
  if (lane == 0) __hip_atomic_fetch_add((LAS unsigned*)f, 1u, __ATOMIC_RELAXED, __HIP_MEMORY_SCOPE_WORKGROUP);
}
__device__ __forceinline__ f32x4 fold8(const f32x4 (&acc)[8], bool rs) {
  f32x4 r;
#pragma unroll
  for (int i = 0; i < 4; ++i) {
    const float a0 = opq(acc[i][0]), a1 = opq(acc[i][1]), b0 = opq(acc[4 + i][0]), b1 = opq(acc[4 + i][1]);
    r[i] = (rs ? b1 : a0) + dpp<ROR8>(rs ? a1 : b0);
  }
  return r;
}
#define ST(i) do {} while (0)
#define STDUMP() do {} while (0)
}

__global__ void __launch_bounds__(NTHR, 2)
fused_decode(const u16*, const u16*, const u16*, const u16*, const u16*, const u16*, const u16*, const u16*, u16*, unsigned char*) {
  v16u ka;
  v4u kb;
  asm volatile("s_load_dwordx16 %0, %2, 0x0\n\ts_load_dwordx4 %1, %2, 0x40" : "=&s"(ka), "=&s"(kb) : "s"(__builtin_amdgcn_kernarg_segment_ptr()));
  __shared__ __attribute__((aligned(16))) unsigned char smem[L_BYTES];
  LAS unsigned char* L = (LAS unsigned char*)smem;
  const int tid = threadIdx.x, lane = tid & 63;
  const int wave = __builtin_amdgcn_readfirstlane(tid >> 6), w4 = wave & 3;
  const int bid = blockIdx.x, xcd = bid & 7, idx = bid >> 3;
  const int g = lane >> 4, c = lane & 15;
  ST(0);
  const unsigned long long tstart = __builtin_amdgcn_s_memrealtime();
  const bool isV = wave < 4;
  unsigned pf;
  {
    unsigned long long pc;
    asm volatile("s_getpc_b64 %0" : "=s"(pc));
    const bool on = wave >= 6 && (idx & PFMASK) == 0;
    pf = __builtin_amdgcn_raw_buffer_load_b32(mkrsrc((const void*)(pc & ~255ull), on ? CODE_BYTES : 0), ((wave & 1) * 64 + lane) * 128, 0, 0);
  }
  volatile LAS unsigned* flag = (volatile LAS unsigned*)(L + L_FLAG);
  if (tid < 16) flag[tid] = 0u;
  if (isV) __builtin_amdgcn_s_setprio(3); else __builtin_amdgcn_s_setprio(2);
  const int b = idx >> 3, split = idx & 7;
  const int sbase = ((b * 8 + xcd) * 1024 + 256 * w4 + split) * 1024;
  const bool rs = (c & 8) != 0;
  const int koff = ((c >> 1) & 1) * 8192 + (c >> 2) * 256 + (2 * g + (c & 1)) * 16;
  const int tile = idx < 16 ? ((xcd + 4) & 7) * 16 + idx : 128 + (idx - 16) * 8 + xcd;
  const bool has_tile = tile < 240;
  v4u R[12];
#define ARGP(T, v, i) ((T)(((unsigned long long)(v)[2 * (i) + 1] << 32) | (v)[2 * (i)]))
  const int cgp = has_tile ? tile >> 1 : 0, k0 = (tile & 1) * 256 + w4 * 64;
  const int ldb = cgp < 64 ? 8192 : 3584;
  const int sb = k0 * ldb + (cgp < 64 ? cgp : cgp < 92 ? cgp - 64 : cgp - 92) * 128;
  const int va = (c >> 2) * 1024 + (c & 3) * 64 + g * 16, vb = ((c >> 3) * 32 + g * 8) * ldb + (c & 7) * 16;
  const int xo = (k0 >> 7) * 256;
  const unsigned wbytes = has_tile ? 512u * ldb : 0u;
  asm volatile("" ::"s"(sb), "s"(ldb), "s"(xo), "s"(wbytes), "s"(sbase), "v"(va), "v"(vb), "v"(koff));
  PIN();
  asm volatile("s_waitcnt lgkmcnt(0)" : "+s"(ka), "+s"(kb)::"memory");
  const u16 *x = ARGP(const u16*, ka, 0), *kc = ARGP(const u16*, ka, 1), *vc = ARGP(const u16*, ka, 2), *wo = ARGP(const u16*, ka, 4), *w2 = ARGP(const u16*, ka, 7);
  u16* out = ARGP(u16*, kb, 0);
  unsigned char* ws = ARGP(unsigned char*, kb, 1);
  unsigned epoch;
  asm volatile("s_load_dword %0, %1, 0x0" : "=&s"(epoch) : "s"(ws));
  unsigned long long tk;
  asm volatile("s_memrealtime %0" : "=&s"(tk));
  const unsigned jit = (((unsigned)(bid * 4 + w4) * 0x9E3779B1u) >> 16) * JIT >> 16;
  const rsrc_t rk = mkrsrc(kc, 4u * 8 * 4096 * 128 * 2);
  if (isV) {
    const u16 *wq = ARGP(const u16*, ka, 3), *w1 = ARGP(const u16*, ka, 5), *w3 = ARGP(const u16*, ka, 6);
    asm("" : "+s"(wq)); asm("" : "+s"(w1)); asm("" : "+s"(w3));
    const u16* wmat = cgp < 64 ? wq : cgp < 92 ? w1 : w3;
    const rsrc_t rx = mkrsrc(x, 4096u), rw = mkrsrc(wmat, wbytes);
#pragma unroll
    for (int j = 0; j < WFIRST; ++j) R[4 + j] = __builtin_amdgcn_raw_buffer_load_b128(rw, vb, sb + j * ldb, AUX_NT);
    PIN();
#pragma unroll
    for (int j = 0; j < 4; ++j) R[j] = __builtin_amdgcn_raw_buffer_load_b128(rx, va, (xo + j * 256) & 768, 0);
    PIN();
#pragma unroll
    for (int j = WFIRST; j < 8; ++j) R[4 + j] = __builtin_amdgcn_raw_buffer_load_b128(rw, vb, sb + j * ldb, AUX_NT);
  } else {
#pragma unroll
    for (int j = 0; j < KPRE; ++j) R[j] = __builtin_amdgcn_raw_buffer_load_b128(rk, koff, sbase + (j >> 1) * 16384 + (j & 1) * 128, AUX_NT);
  }
  PIN();
  __builtin_amdgcn_s_barrier();
  asm volatile("" ::: "memory");
  PIN();
  unsigned tag;
  unsigned long long tk2;
  asm volatile("s_waitcnt lgkmcnt(0)\n\ts_add_u32 %0, %2, 1\n\ts_mov_b64 %1, %3" : "=&s"(tag), "=&s"(tk2) : "s"(epoch), "s"(tk) : "memory", "scc");
  unsigned tz = (unsigned)tk2;
  const Tag T = mktag(tag);
  const rsrc_t rws = mkrsrc(ws, WS_BYTES);

  if (isV) {
    {
      float s = 0.f;
#pragma unroll
      for (int i = 0; i < 4; ++i)
#pragma unroll
        for (int k = 0; k < 4; ++k) s += lo16(R[i][k]) + hi16(R[i][k]);
      s += dpp<XOR1>(s); s += dpp<XOR2>(s);
      const float mean = xrow_sum(s) * (1.0f / 512.0f);
      float q = 0.f;
#pragma unroll
      for (int i = 0; i < 4; ++i)
#pragma unroll
        for (int k = 0; k < 4; ++k) {
          const float d0 = lo16(R[i][k]) - mean, d1 = hi16(R[i][k]) - mean;
          q = fmaf(d0, d0, q); q = fmaf(d1, d1, q);
        }
      q += dpp<XOR1>(q); q += dpp<XOR2>(q);
      const float var = rbf(xrow_sum(q) * (1.0f / 512.0f));
      const float den = rbf(__builtin_amdgcn_sqrtf(rbf(var + 1e-5f)));
      const float inv = __builtin_amdgcn_rcpf(den), mb = rbf(mean);
      v4u A;
#pragma unroll
      for (int k = 0; k < 4; ++k) {
        const unsigned d = pk2(lo16(R[0][k]) - mb, hi16(R[0][k]) - mb);
        const unsigned a = pk2(lo16(d) * inv, hi16(d) * inv), a2 = dppu<0xEE>(a);
        A[k] = (w4 & 1) ? a2 : a;
      }
      ST(1);
      f32x4 acc[8];
      {
        v4u bf[8];
#pragma unroll
        for (int t = 0; t < 4; ++t) {
#pragma unroll
          for (int i = 0; i < 8; ++i) {
            bf[i][t] = permsel(R[4 + 2 * t], R[5 + 2 * t], i);
            if (t == 3) acc[i] = mfma(A, bf[i], f32x4{0.f, 0.f, 0.f, 0.f});
          }
          PIN();
        }
      }
      *(LAS f32x4*)(L + L_PX + w4 * 1024 + lane * 16) = fold8(acc, rs);
      LGKM0();
      unsigned old = 0u;
      if (lane == 0) old = __hip_atomic_fetch_add((LAS unsigned*)(flag + 1), 1u, __ATOMIC_RELAXED, __HIP_MEMORY_SCOPE_WORKGROUP);
      if (__builtin_amdgcn_readfirstlane(old) == 3u && has_tile) {
        asm volatile("" ::: "memory");
        const f32x4 o = (*(LAS f32x4*)(L + L_PX + lane * 16) + *(LAS f32x4*)(L + L_PX + 1024 + lane * 16)) +
                        (*(LAS f32x4*)(L + L_PX + 2048 + lane * 16) + *(LAS f32x4*)(L + L_PX + 3072 + lane * 16));
        st16(rws, WS_PG + tile * 1024 + lane * 16, unit(o, T));
      }
      PIN();
      ST(2);
    }
    __builtin_amdgcn_s_setprio(0);
    if (w4 == 0) {
      pace(tz, CKT);
      const v2u ck = __builtin_amdgcn_raw_buffer_load_b64(rws, (lane & 7) * 128, WS_CK, AUX_SC1);
      const int dt = mean_offset(ck, tz, tag);
      if (lane == 0) flag[8] = (tz + dt + GOFF) | 1u;
    }
    lds_wait<4>(flag + 8, 1u);
    tz = __builtin_amdgcn_readfirstlane(flag[8]) - jit;
    const rsrc_t rv = mkrsrc(vc, 4u * 8 * 4096 * 128 * 2);
    const int voff = lane * 16;
    v4u V[32];
    v4u bf[4][8];
#define VPERM(pr) do { _Pragma("unroll") for (int i = 0; i < 8; ++i) bf[(pr) >> 2][i][(pr) & 3] = permsel(V[2 * (pr)], V[2 * (pr) + 1], i); } while (0)
#pragma unroll
    for (int j = 0; j < 32; ++j) {
      if (j >= VD2) VMCNT(VD2 - 1);
      if ((j & 1) == 0) pace(tz, PT(32 + j));
      V[j] = __builtin_amdgcn_raw_buffer_load_b128(rv, voff, sbase + ((j & 24) + 4 * (j & 1) + ((j >> 1) & 3)) * 8192, AUX_NT);
      PIN();
      if (j >= VD2 + 1 && ((j - VD2) & 1)) { VPERM((j - VD2) >> 1); PIN(); }
    }
    lds_inc(flag + 4, lane);
    ST(3);
    lds_wait<1>(flag + 3, 4u);
    v4u P[4];
#pragma unroll
    for (int G = 0; G < 4; ++G) P[G] = *(LAS v4u*)(L + L_P + w4 * 1024 + G * 256 + (lane & ~3) * 4);
    ST(4);
    f32x4 acc[8];
#pragma unroll
    for (int i = 0; i < 8; ++i) acc[i] = f32x4{0.f, 0.f, 0.f, 0.f};
    constexpr int DONE = (32 - VD2) >> 1;
#pragma unroll
    for (int G = 0; G < 4; ++G) {
#pragma unroll
      for (int t = 0; t < 4; ++t)
        if (4 * G + t >= DONE) { VPERM(4 * G + t); if (t < 3) PIN(); }
#pragma unroll
      for (int i = 0; i < 8; ++i) acc[i] = mfma(P[G], bf[G][i], acc[i]);
      PIN();
    }
    *(LAS f32x4*)(L + L_ORED + w4 * 2048 + lane * 16) = f32x4{acc[0][0], acc[1][0], acc[2][0], acc[3][0]};
    *(LAS f32x4*)(L + L_ORED + w4 * 2048 + 1024 + lane * 16) = f32x4{acc[4][0], acc[5][0], acc[6][0], acc[7][0]};
    ST(5);
    wg_barrier();
    ST(6);
    if (lane < 32) {
      const int ro = (lane & 1) * 1024 + (w4 * 16 + (lane >> 1)) * 16;
      const f32x4 o = (*(LAS f32x4*)(L + L_ORED + ro) + *(LAS f32x4*)(L + L_ORED + 2048 + ro)) +
                      (*(LAS f32x4*)(L + L_ORED + 4096 + ro) + *(LAS f32x4*)(L + L_ORED + 6144 + ro));
      st16(rws, WS_OG + ((b * 8 + xcd) * 4 + w4) * 4096 + g * 2048 + split * 256 + c * 16, unit(o, T));
    }
    ST(7);
    wg_barrier();
    asm volatile("" ::"v"(pf));
    STDUMP();
    return;
  }

  v4u K[32];
#pragma unroll
  for (int j = 0; j < KPRE; ++j) K[j] = R[j];
  if (w4 == 0 && idx == 0 && lane == 0) __builtin_amdgcn_raw_buffer_store_b64(v2u{tz, tag}, rws, WS_CK + xcd * 128, 0, AUX_SC1);
  tz -= jit;
  v2u ckk;
  PIN();
#pragma unroll
  for (int j = KPRE; j < 32; ++j) {
    if (CKJ && j == CKJ + 6) {
      const int dt = __builtin_amdgcn_readfirstlane(mean_offset(ckk, tz + jit, tag));
      if (dt < 0) tz += dt;
      PIN();
    }
    if ((j & 1) == 0 && PT(j) > 0) pace(tz, PT(j));
    if (CKJ && j == CKJ) ckk = __builtin_amdgcn_raw_buffer_load_b64(rws, (lane & 7) * 128, WS_CK, AUX_SC1);
    K[j] = __builtin_amdgcn_raw_buffer_load_b128(rk, koff, sbase + (j >> 1) * 16384 + (j & 1) * 128, AUX_NT);
    PIN();
  }
  {
    ST(1);
    {
      const int off[1] = {WS_PG + (xcd * 16 + 4 * w4 + g) * 1024 + b * 256 + c * 16};
      v4u gv[1] = {ld16(rws, off[0])};
      asm volatile("" ::"v"(pf));
      settle<1, 8>(rws, off, gv, T);
      const float q0 = fold16(uval(gv[0][0]), uval(gv[0][2])), q1 = fold16(uval(gv[0][1]), uval(gv[0][3]));
      *(LAS unsigned*)(L + L_QS + (128 * w4 + 64 * (g >> 1) + 8 * (c & 7) + 4 * (c >> 3) + 2 * (g & 1)) * 2) = pk2(q0, q1);
      lds_inc(flag, lane);
      lds_wait<1>(flag, 4u);
    }
    ST(2);
    v4u qf[2][2];
    {
      const bool a1 = (c & 2) != 0;
#pragma unroll
      for (int p = 0; p < 2; ++p) {
        const v4u t = *(LAS v4u*)(L + L_QS + (c >> 2) * 256 + (8 * p + 2 * g + (c & 1)) * 16);
#pragma unroll
        for (int k = 0; k < 4; ++k) { qf[p][0][k] = a1 ? 0u : t[k]; qf[p][1][k] = a1 ? t[k] : 0u; }
      }
    }
    float s[8];
    {
      f32x4 d[8];
#pragma unroll
      for (int n = 0; n < 8; ++n) d[n] = f32x4{0.f, 0.f, 0.f, 0.f};
#pragma unroll
      for (int z = 0; z < 4; ++z) {
#pragma unroll
        for (int n = 0; n < 8; ++n) d[n] = mfma(K[4 * n + z], qf[z & 1][z >> 1], d[n]);
        PIN();
      }
      const bool u1 = (c & 1) != 0;
#pragma unroll
      for (int n = 0; n < 8; ++n) {
        const float d0 = opq(d[n][0]), d1 = opq(d[n][1]), d2 = opq(d[n][2]), d3 = opq(d[n][3]);
        s[n] = (u1 ? d3 : d0) + dpp<XOR1>(u1 ? d1 : d2);
      }
    }
    float m = -INFINITY;
#pragma unroll
    for (int n = 0; n < 8; n += 2) {
      const unsigned r1 = pk2(s[n], s[n + 1]);
      const unsigned r2 = pk2(lo16(r1) * SCALE, hi16(r1) * SCALE);
      s[n] = lo16(r2); s[n + 1] = hi16(r2);
      m = fmaxf(m, fmaxf(s[n], s[n + 1]));
    }
    m = fmaxf(m, dpp<XOR1>(m)); m = fmaxf(m, dpp<XOR2>(m));
    m = xrow_max(m) * LOG2E;
    if (g == 0 && (c & 3) == 0) *(LAS float*)(L + L_MX + ((c >> 2) * 4 + w4) * 4) = m;
    lds_inc(flag + 2, lane);
    lds_wait<1>(flag + 2, 4u);
    {
      const f32x4 ma = *(LAS f32x4*)(L + L_MX + (c >> 2) * 16);
      const float m2 = cut(fmaxf(fmaxf(ma[0], ma[1]), fmaxf(ma[2], ma[3])));
      float l = 0.f;
#pragma unroll
      for (int G = 0; G < 4; ++G) {
        const float p0 = __builtin_amdgcn_exp2f(fmaf(s[2 * G], LOG2E, -m2)), p1 = __builtin_amdgcn_exp2f(fmaf(s[2 * G + 1], LOG2E, -m2));
        l += p0 + p1;
        *(LAS unsigned*)(L + L_P + w4 * 1024 + G * 256 + lane * 4) = pk2(p0, p1);
      }
      l += dpp<XOR1>(l); l += dpp<XOR2>(l);
      l = xrow_sum(l);
      if (g == 0 && (c & 3) == 0) *(LAS float*)(L + L_LS + ((c >> 2) * 4 + w4) * 4) = l;
      lds_inc(flag + 3, lane);
    }
    ST(3);
  }
  const int kvc = (2 * (xcd & 3) + 2 + 4 * (xcd >> 2) + (idx >> 4)) & 7, r8 = (idx >> 1) & 7, cg = 2 * (xcd >> 2) + (idx & 1);
  const int cgf = 2 * (1 - (xcd >> 2)) + (xcd & 1), bf = 2 * ((xcd >> 1) & 1) + (idx >> 4), fa = idx & 15;
  const int rg = kvc * 8 + r8;
  const int fc = 2 * (fa & 7) + (lane & 1);
  const int col = 128 * cgf + 64 * (fa >> 3) + 8 * (fc & 7) + 4 * (fc >> 3) + 2 * (g >> 1) + (g & 1);
  v4u W[12];
  unsigned xr = 0u;
  if (w4 == 0) {
    xr = __builtin_amdgcn_raw_buffer_load_b16(mkrsrc(x, 4096u), (bf * 512 + col) * 2, 0, 0);
    lds_wait<1>(flag + 3, 4u);
    if (lane < 4) {
      const f32x4 ma = *(LAS f32x4*)(L + L_MX + lane * 16), la = *(LAS f32x4*)(L + L_LS + lane * 16);
      st16(rws, WS_ML + ((b * 8 + xcd) * 4 + lane) * 128 + split * 16,
           unit(f32x4{fmaxf(fmaxf(ma[0], ma[1]), fmaxf(ma[2], ma[3])), (la[0] + la[1]) + (la[2] + la[3]), 0.f, 0.f}, T));
    }
  } else if (w4 == 1) {
    const int qd = c & 7, f = 28 * rg + 4 * (qd < 7 ? qd : 6);
    const int vo = WS_PG + (128 + 2 * (f >> 6) + (c >> 3)) * 1024 + g * 256 + (((f >> 2) & 1) * 8 + ((f & 63) >> 3)) * 16;
    const int off[2] = {vo, vo + 56 * 1024};
    v4u gv[2] = {ld16(rws, off[0]), ld16(rws, off[1])};
    settle<2, 16>(rws, off, gv, T);
    float hv[4];
#pragma unroll
    for (int k = 0; k < 4; ++k) {
      const float g0 = uval(gv[0][k]), u0 = uval(gv[1][k]);
      const float gt = rbf(g0 + dpp<ROR8>(g0)), up = rbf(u0 + dpp<ROR8>(u0));
      const float sl = rbf(gt * __builtin_amdgcn_rcpf(1.0f + __builtin_amdgcn_exp2f(-gt * LOG2E)));
      hv[k] = sl * up;
    }
    if (c < 8) *(LAS v2u*)(L + L_HA + g * 64 + c * 8) = qd < 7 ? v2u{pk2(hv[0], hv[1]), pk2(hv[2], hv[3])} : v2u{0u, 0u};
  }
  float w0, w1_;
  {
    lds_wait<4>(flag + 8, 1u);
    tz = __builtin_amdgcn_readfirstlane(flag[8]) - jit;
    pace(tz, PEND - MLT);
    const int mloff[1] = {WS_ML + ((w4 * 8 + kvc) * 4 + (r8 >> 1)) * 128 + (4 * (c & 1) + g) * 16};
    v4u ml[1] = {ld16(rws, mloff[0])};
    settle<1, 4>(rws, mloff, ml, T);
    const float ms = uval(ml[0][0]);
    const float M = xrow_max(fmaxf(ms, dpp<XOR1>(ms)));
    float wa = __builtin_amdgcn_exp2f(ms - M);
    float Ls = wa * uval(ml[0][1]);
    Ls = xrow_sum(Ls + dpp<XOR1>(Ls));
    wa *= __builtin_amdgcn_rcpf(Ls);
    w0 = dpp<0xA0>(wa); w1_ = dpp<0xF5>(wa);
    asm volatile("" : "+v"(w0), "+v"(w1_));
  }
  if (w4 >= 2) {
    __builtin_amdgcn_s_setprio(0);
    const rsrc_t rwo = mkrsrc(wo, 4096u * 512 * 2), rw2 = mkrsrc(w2, 1792u * 512 * 2);
    const int colb = (128 * cg + 64 * (w4 - 2)) * 2;
    const int vo = ((c >> 3) * 32 + g * 8) * 1024 + (c & 7) * 16;
    const int vo2 = (rs && g == 3) ? 0x7ff00000 : ((c >> 3) * 16 + g * 4) * 1024 + (c & 7) * 16;
    asm volatile("" ::"v"(vo), "v"(vo2));
#define WOLOAD(n) do { if ((n) < 4) W[8 + (n)] = __builtin_amdgcn_raw_buffer_load_b128(rw2, vo2, (28 * rg + (n)) * 1024 + colb, AUX_NT); \
                       else W[(n) - 4] = __builtin_amdgcn_raw_buffer_load_b128(rwo, vo, (64 * rg + (n) - 4) * 1024 + colb, AUX_NT); } while (0)
    if (WOE) {
      pace(tz, PEND + WOT0);
#pragma unroll
      for (int n = 0; n < WOE; ++n) WOLOAD(n);
      PIN();
    }
    if (WOE < 12) {
      lds_wait<2>(flag + 4, 4u);
      pace(tz, PEND + WOT);
#pragma unroll
      for (int n = WOE; n < 12; ++n) WOLOAD(n);
    }
    PIN();
  }
  v4u Bo[8];
  f32x4 acc[8];
  ST(5);
  wg_barrier();
  wg_barrier();
  {
    __builtin_amdgcn_s_sleep(PDELAY1);
    if (T1) pace(tz + jit, PEND + T1);
    const int so = WS_OG + ((w4 * 8 + kvc) * 8 + r8) * 2048 + lane * 16;
    const int off[2] = {so, so + 1024};
    v4u gv[2] = {ld16(rws, off[0]), ld16(rws, off[1])};
    PIN();
    if (w4 >= 2) {
      const v2u ha = *(LAS v2u*)(L + L_HA + (c >> 2) * 64 + (c & 1) * 32 + g * 8);
#pragma unroll
      for (int i = 0; i < 8; ++i)
        acc[i] = mfma(v4u{ha[0], ha[1], 0u, 0u}, v4u{permsel(W[8], W[9], i), permsel(W[10], W[11], i), 0u, 0u}, f32x4{0.f, 0.f, 0.f, 0.f});
      PIN();
#pragma unroll
      for (int t = 0; t < 4; ++t) {
#pragma unroll
        for (int i = 0; i < 8; ++i) Bo[i][t] = permsel(W[2 * t], W[2 * t + 1], i);
        PIN();
      }
    }
    PIN();
    settle<2, POLL_SLEEP>(rws, off, gv, T);
    float o[4];
#pragma unroll
    for (int k = 0; k < 4; ++k) o[k] = w0 * uval(gv[0][k]) + w1_ * uval(gv[1][k]);
    const float r = fold16(fold32(o[0], o[2]), fold32(o[1], o[3]));
    *(LAS u16*)(L + L_OA + w4 * 128 + (4 * c + g) * 2) = (u16)pk2(r, r);
  }
  ST(6);
  wg_barrier();
  if (w4 >= 2) {
    const v4u a0 = *(LAS v4u*)(L + L_OA + (c >> 2) * 128 + (c & 1) * 64 + g * 16);
#pragma unroll
    for (int i = 0; i < 8; ++i) acc[i] = mfma(a0, Bo[i], acc[i]);
    st16(rws, WS_YG + ((cg * 64 + rg) * 2 + (w4 - 2)) * 1024 + lane * 16, unit(fold8(acc, rs), T));
    wg_barrier();
  } else if (w4 == 0) {
    wg_barrier();
    __builtin_amdgcn_s_sleep(PDELAY);
    if (T2) pace(tz + jit, PEND + T2);
    const int vy = WS_YG + (cgf * 64 + (lane >> 1)) * 2048 + (fa >> 3) * 1024 + bf * 256 + fc * 16;
    const int off[2] = {vy, vy + 32 * 2048};
    v4u y[2] = {ld16(rws, off[0]), ld16(rws, off[1])};
    settle<2, POLL2_SLEEP>(rws, off, y, T);
    float o[4];
#pragma unroll
    for (int k = 0; k < 4; ++k) o[k] = uval(y[0][k]) + uval(y[1][k]);
    float r = fold16(fold32(o[0], o[2]), fold32(o[1], o[3]));
    r += dpp<XOR2>(r); r += dpp<ROR4>(r); r += dpp<ROR8>(r);
    r += lo16(xr);
    if (c < 2) __builtin_amdgcn_raw_buffer_store_b16((u16)pk2(r, r), mkrsrc(out, 4096u), (bf * 512 + col) * 2, 0, OAUX);
    if (bid == 0 && lane == 0) __builtin_amdgcn_raw_buffer_store_b32(tag, rws, 0, WS_E, AUX_SC1);
    if (bid == 0) {
      const v2u ck = __builtin_amdgcn_raw_buffer_load_b64(rws, (lane & 7) * 128, WS_CK, AUX_SC1);
      int d = (int)(ck[0] - (unsigned)tk2), mn = d, mx = d, sm = d;
      mn = min(mn, (int)dppu<XOR1>(mn)); mn = min(mn, (int)dppu<XOR2>(mn)); mn = min(mn, (int)dppu<ROR4>(mn));
      mx = max(mx, (int)dppu<XOR1>(mx)); mx = max(mx, (int)dppu<XOR2>(mx)); mx = max(mx, (int)dppu<ROR4>(mx));
      sm += (int)dppu<XOR1>(sm); sm += (int)dppu<XOR2>(sm); sm += (int)dppu<ROR4>(sm);
      const unsigned long long lim = 4000 + (unsigned long long)__builtin_amdgcn_readfirstlane(-8 * mn);
      for (int i = 0; i < 1000000; ++i) { if (__builtin_amdgcn_s_memrealtime() - tstart >= lim) break; __builtin_amdgcn_s_sleep(1); } }
  } else if (STAMP) {
    wg_barrier();
  }
  ST(7);
  STDUMP();
}

extern "C" void kernel_launch(void* const* d_in, const int* in_sizes, int n_in, void* d_out, int out_size, void* d_ws, size_t ws_size,
                              hipStream_t stream) {
  (void)in_sizes; (void)n_in; (void)out_size; (void)ws_size;
  fused_decode<<<256, NTHR, 0, stream>>>((const u16*)d_in[0], (const u16*)d_in[1], (const u16*)d_in[2], (const u16*)d_in[3], (const u16*)d_in[4],
                                         (const u16*)d_in[6], (const u16*)d_in[7], (const u16*)d_in[8], (u16*)d_out, (unsigned char*)d_ws);
}
```

```cpp
#include <hip/hip_runtime.h>
#include <stdint.h>

typedef unsigned short u16;
typedef unsigned v4u __attribute__((ext_vector_type(4)));
typedef unsigned v2u __attribute__((ext_vector_type(2)));
typedef unsigned v16u __attribute__((ext_vector_type(16)));
typedef __bf16 bf16x8 __attribute__((ext_vector_type(8)));
typedef __bf16 bf16x2 __attribute__((ext_vector_type(2)));
typedef float f32x4 __attribute__((ext_vector_type(4)));
typedef float f32x2 __attribute__((ext_vector_type(2)));
#define LAS __attribute__((address_space(3)))

namespace {
constexpr int AUX_NT = 2, AUX_SC1 = 16, RSRC_W3 = 0x00020000;
constexpr int NTHR = 512;
constexpr unsigned SPIN_CAP = 1u << 16;
constexpr int PEND = 1025, PD = 145;
constexpr int pt(int r) { return PEND - ((63 - r) * PD) / 10; }
constexpr int KPRE = 4;
constexpr int JIT = 60;
constexpr int CKT = 320, GOFF = 6;
constexpr int MLT = 80, WOT = 30;
constexpr int VD = 16;
constexpr int PDELAY1 = 12, PDELAY2 = 12;
constexpr unsigned CODE_BYTES = 16320;
constexpr int WS_E = 0;
constexpr int WS_PG = 4096;
constexpr int WS_ML = WS_PG + 240 * 1024;
constexpr int WS_CK = WS_ML + 128 * 128;
constexpr int WS_OG = WS_CK + 16 * 128;
constexpr int WS_YG = WS_OG + 128 * 4096;
constexpr int WS_BYTES = WS_YG + 4 * 64 * 2048;
constexpr int L_ORED = 0;
constexpr int L_PX = L_ORED + 8192;
constexpr int L_P = L_PX + 4096;
constexpr int L_QS = L_P + 4096;
constexpr int L_MX = L_QS + 1024;
constexpr int L_LS = L_MX + 64;
constexpr int L_OA = L_LS + 64;
constexpr int L_HA = L_OA + 512;
constexpr int L_FLAG = L_HA + 256;
constexpr int L_BYTES = L_FLAG + 64;

constexpr float LOG2E = 1.4426950408889634f;
constexpr float SCALE = 0.08838834764831845f;

__device__ __forceinline__ float asf(unsigned u) { return __builtin_bit_cast(float, u); }
__device__ __forceinline__ unsigned asu(float f) { return __builtin_bit_cast(unsigned, f); }
__device__ __forceinline__ unsigned pk2(float lo, float hi) {
  f32x2 p = {lo, hi};
  return __builtin_bit_cast(unsigned, __builtin_convertvector(p, bf16x2));
}
__device__ __forceinline__ float lo16(unsigned u) { return asf(u << 16); }
__device__ __forceinline__ float hi16(unsigned u) { return asf(u & 0xffff0000u); }
__device__ __forceinline__ float rbf(float f) { return lo16(pk2(f, f)); }
template <int CTRL> __device__ __forceinline__ unsigned dppu(unsigned x) { return (unsigned)__builtin_amdgcn_mov_dpp((int)x, CTRL, 0xf, 0xf, true); }
template <int CTRL> __device__ __forceinline__ float dpp(float x) { return asf(dppu<CTRL>(asu(x))); }
constexpr int XOR1 = 0xB1, XOR2 = 0x4E, ROR8 = 0x128, ROR4 = 0x124;
__device__ __forceinline__ float opq(float x) { asm("" : "+v"(x)); return x; }
__device__ __forceinline__ float fold32(float a, float b) {
  auto t = __builtin_amdgcn_permlane32_swap(asu(a), asu(b), false, false);
  return asf(t[0]) + asf(t[1]);
}
__device__ __forceinline__ float fold16(float a, float b) {
  auto t = __builtin_amdgcn_permlane16_swap(asu(a), asu(b), false, false);
  return asf(t[0]) + asf(t[1]);
}
__device__ __forceinline__ float xrow_sum(float x) { x = fold16(x, x); return fold32(x, x); }
__device__ __forceinline__ float xrow_max(float x) {
  auto s = __builtin_amdgcn_permlane16_swap(asu(x), asu(x), false, false);
  x = fmaxf(asf(s[0]), asf(s[1]));
  auto t = __builtin_amdgcn_permlane32_swap(asu(x), asu(x), false, false);
  return fmaxf(asf(t[0]), asf(t[1]));
}
__device__ __forceinline__ f32x4 mfma(v4u a, v4u b, f32x4 c) {
  return __builtin_amdgcn_mfma_f32_16x16x32_bf16(__builtin_bit_cast(bf16x8, a), __builtin_bit_cast(bf16x8, b), c, 0, 0, 0);
}
__device__ __forceinline__ unsigned permsel(const v4u& e, const v4u& o, int i) {
  return __builtin_amdgcn_perm(o[i >> 1], e[i >> 1], (i & 1) ? 0x07060302u : 0x05040100u);
}
#define PIN() __builtin_amdgcn_sched_barrier(0)
#define LGKM0() asm volatile("s_waitcnt lgkmcnt(0)" ::: "memory")
#define VMCNT(n) asm volatile("s_waitcnt vmcnt(%0)" ::"i"(n) : "memory")
__device__ __forceinline__ void wg_barrier() {
  PIN();
  LGKM0();
  __builtin_amdgcn_s_barrier();
  asm volatile("" ::: "memory");
  PIN();
}
typedef __amdgpu_buffer_rsrc_t rsrc_t;
__device__ __forceinline__ rsrc_t mkrsrc(const void* p, unsigned bytes) { return __builtin_amdgcn_make_buffer_rsrc((void*)p, 0, (int)bytes, RSRC_W3); }
struct Tag { unsigned s[4]; };
__device__ __forceinline__ Tag mktag(unsigned t) {
  return Tag{{t & 1023u, (t + (t >> 10) + 341u) & 1023u, (t + (t >> 20) + 682u) & 1023u, (t + (t >> 30) + 171u) & 1023u}};
}
__device__ __forceinline__ float cut(float a) { return asf((asu(a) + 512u) & ~1023u); }
__device__ __forceinline__ v4u unit(f32x4 v, const Tag& t) {
  return v4u{asu(cut(v[0])) | t.s[0], asu(cut(v[1])) | t.s[1], asu(cut(v[2])) | t.s[2], asu(cut(v[3])) | t.s[3]};
}
__device__ __forceinline__ float uval(unsigned u) { return asf(u & ~1023u); }
__device__ __forceinline__ v4u unit_se(f32x4 v, const Tag& t) {
  const unsigned e = asu(fmaxf(fmaxf(fabsf(v[0]), fabsf(v[1])), fmaxf(fabsf(v[2]), fabsf(v[3])))) >> 23;
  v4u r;
#pragma unroll
  for (int k = 0; k < 4; ++k) {
    const int m = min((int)__builtin_rintf(__builtin_amdgcn_ldexpf(v[k], 145 - (int)e)), 0x7ffff);
    r[k] = ((unsigned)m << 12) | (((e >> (2 * k)) & 3u) << 10) | t.s[k];
  }
  return r;
}
__device__ __forceinline__ f32x4 uval_se(const v4u& u) {
  const int e = (int)(((u[0] >> 10) & 3u) | ((u[1] >> 8) & 12u) | ((u[2] >> 6) & 48u) | ((u[3] >> 4) & 192u)) - 145;
  f32x4 r;
#pragma unroll
  for (int k = 0; k < 4; ++k) r[k] = __builtin_amdgcn_ldexpf((float)((int)u[k] >> 12), e);
  return r;
}
__device__ __forceinline__ bool unit_bad(const v4u& v, const Tag& t) {
  return ((((v[0] ^ t.s[0]) | (v[1] ^ t.s[1])) | ((v[2] ^ t.s[2]) | (v[3] ^ t.s[3]))) & 1023u) != 0u;
}
__device__ __forceinline__ void st16(rsrc_t rs, int off, v4u v) { __builtin_amdgcn_raw_buffer_store_b128(v, rs, off, 0, AUX_SC1); }
__device__ __forceinline__ v4u ld16(rsrc_t rs, int off) { return __builtin_amdgcn_raw_buffer_load_b128(rs, off, 0, AUX_SC1); }
template <int N, int SLEEP>
__device__ __forceinline__ void settle(rsrc_t rs, const int (&off)[N], v4u (&v)[N], const Tag& t) {
  for (unsigned sp = 0; sp < SPIN_CAP; ++sp) {
    bool bad[N], any = false;
#pragma unroll
    for (int k = 0; k < N; ++k) { bad[k] = unit_bad(v[k], t); any |= bad[k]; }
    if (__builtin_amdgcn_ballot_w64(any) == 0ull) break;
    __builtin_amdgcn_s_sleep(SLEEP);
    asm volatile("" ::: "memory");
#pragma unroll
    for (int k = 0; k < N; ++k)
      if (bad[k]) v[k] = ld16(rs, off[k]);
  }
}
template <int SLEEP>
__device__ __forceinline__ void lds_wait(volatile LAS unsigned* f, unsigned want) {
#pragma clang loop unroll(disable)
  for (unsigned sp = 0; sp < (1u << 24); ++sp) {
    if ((unsigned)__builtin_amdgcn_readfirstlane(*f) >= want) break;
    __builtin_amdgcn_s_sleep(SLEEP);
  }
  asm volatile("" ::: "memory");
}
__device__ __forceinline__ void pace(unsigned tz, int ticks) {
#pragma clang loop unroll(disable)
  for (int sp = 0; sp < 4096; ++sp) {
    if ((int)((unsigned)__builtin_amdgcn_s_memrealtime() - tz) >= ticks) break;
    __builtin_amdgcn_s_sleep(1);
  }
  PIN();
}
__device__ __forceinline__ int mean_offset(v2u ck, unsigned mine, unsigned tag) {
  int dt = (int)(ck[0] - mine);
  const bool ok = ck[1] == tag && dt > -300 && dt < 300;
  dt = ok ? dt : 0;
  dt += (int)dppu<XOR1>(dt); dt += (int)dppu<XOR2>(dt); dt += (int)dppu<ROR4>(dt);
  const int n = __builtin_popcount((unsigned)__builtin_amdgcn_ballot_w64(ok) & 0xffu);
  return (int)((float)dt * __builtin_amdgcn_rcpf((float)(n > 0 ? n : 1)));
}
__device__ __forceinline__ void lds_inc(volatile LAS unsigned* f, int lane) {
  LGKM0();
  if (lane == 0) __hip_atomic_fetch_add((LAS unsigned*)f, 1u, __ATOMIC_RELAXED, __HIP_MEMORY_SCOPE_WORKGROUP);
}
__device__ __forceinline__ f32x4 fold8(const f32x4 (&acc)[8], bool rs) {
  f32x4 r;
#pragma unroll
  for (int i = 0; i < 4; ++i) {
    const float a0 = opq(acc[i][0]), a1 = opq(acc[i][1]), b0 = opq(acc[4 + i][0]), b1 = opq(acc[4 + i][1]);
    r[i] = (rs ? b1 : a0) + dpp<ROR8>(rs ? a1 : b0);
  }
  return r;
}
}

__global__ void __launch_bounds__(NTHR, 2)
fused_decode(const u16*, const u16*, const u16*, const u16*, const u16*, const u16*, const u16*, const u16*, u16*, unsigned char*) {
  v16u ka;
  v4u kb;
  asm volatile("s_load_dwordx16 %0, %2, 0x0\n\ts_load_dwordx4 %1, %2, 0x40" : "=&s"(ka), "=&s"(kb) : "s"(__builtin_amdgcn_kernarg_segment_ptr()));
  __shared__ __attribute__((aligned(16))) unsigned char smem[L_BYTES];
  LAS unsigned char* L = (LAS unsigned char*)smem;
  const int tid = threadIdx.x, lane = tid & 63;
  const int wave = __builtin_amdgcn_readfirstlane(tid >> 6), w4 = wave & 3;
  const int bid = blockIdx.x, xcd = bid & 7, idx = bid >> 3;
  const int g = lane >> 4, c = lane & 15;
  const bool isV = wave < 4;
  unsigned pf;
  {
    unsigned long long pc;
    asm volatile("s_getpc_b64 %0" : "=s"(pc));
    const bool on = wave >= 6 && (idx & 3) == 0;
    pf = __builtin_amdgcn_raw_buffer_load_b32(mkrsrc((const void*)(pc & ~255ull), on ? CODE_BYTES : 0u), ((wave & 1) * 64 + lane) * 128, 0, 0);
  }
  volatile LAS unsigned* flag = (volatile LAS unsigned*)(L + L_FLAG);
  if (tid < 16) flag[tid] = 0u;
  if (isV) __builtin_amdgcn_s_setprio(3); else __builtin_amdgcn_s_setprio(2);
  const int b = idx >> 3, split = idx & 7;
  const int sbase = ((b * 8 + xcd) * 1024 + 256 * w4 + split) * 1024;
  const bool rs = (c & 8) != 0;
  const int koff = ((c >> 1) & 1) * 8192 + (c >> 2) * 256 + (2 * g + (c & 1)) * 16;
  const int tile = idx < 16 ? ((xcd + 4) & 7) * 16 + idx : 128 + (idx - 16) * 8 + xcd;
  const bool has_tile = tile < 240;
  v4u R[12];
#define ARGP(T, v, i) ((T)(((unsigned long long)(v)[2 * (i) + 1] << 32) | (v)[2 * (i)]))
  const int cgp = has_tile ? tile >> 1 : 0, k0 = (tile & 1) * 256 + w4 * 64;
  const int ldb = cgp < 64 ? 8192 : 3584;
  const int sb = k0 * ldb + (cgp < 64 ? cgp : cgp < 92 ? cgp - 64 : cgp - 92) * 128;
  const int va = (c >> 2) * 1024 + (c & 3) * 64 + g * 16, vb = ((c >> 3) * 32 + g * 8) * ldb + (c & 7) * 16;
  const int xo = (k0 >> 7) * 256;
  const unsigned wbytes = has_tile ? 512u * ldb : 0u;
  asm volatile("" ::"s"(sb), "s"(ldb), "s"(xo), "s"(wbytes), "s"(sbase), "v"(va), "v"(vb), "v"(koff));
  PIN();
  asm volatile("s_waitcnt lgkmcnt(0)" : "+s"(ka), "+s"(kb)::"memory");
  const u16 *x = ARGP(const u16*, ka, 0), *kc = ARGP(const u16*, ka, 1), *vc = ARGP(const u16*, ka, 2), *wo = ARGP(const u16*, ka, 4), *w2 = ARGP(const u16*, ka, 7);
  u16* out = ARGP(u16*, kb, 0);
  unsigned char* ws = ARGP(unsigned char*, kb, 1);
  unsigned epoch;
  asm volatile("s_load_dword %0, %1, 0x0" : "=&s"(epoch) : "s"(ws));
  unsigned long long tk;
  asm volatile("s_memrealtime %0" : "=&s"(tk));
  const unsigned jit = (((unsigned)(bid * 4 + w4) * 0x9E3779B1u) >> 16) * JIT >> 16;
  const rsrc_t rk = mkrsrc(kc, 4u * 8 * 4096 * 128 * 2);
  if (isV) {
    const u16 *wq = ARGP(const u16*, ka, 3), *w1 = ARGP(const u16*, ka, 5), *w3 = ARGP(const u16*, ka, 6);
    asm("" : "+s"(wq)); asm("" : "+s"(w1)); asm("" : "+s"(w3));
    const u16* wmat = cgp < 64 ? wq : cgp < 92 ? w1 : w3;
    const rsrc_t rx = mkrsrc(x, 4096u), rw = mkrsrc(wmat, wbytes);
#pragma unroll
    for (int j = 0; j < 4; ++j) R[j] = __builtin_amdgcn_raw_buffer_load_b128(rx, va, (xo + j * 256) & 768, 0);
    PIN();
#pragma unroll
    for (int j = 0; j < 8; ++j) R[4 + j] = __builtin_amdgcn_raw_buffer_load_b128(rw, vb, sb + j * ldb, AUX_NT);
  } else {
#pragma unroll
    for (int j = 0; j < KPRE; ++j) R[j] = __builtin_amdgcn_raw_buffer_load_b128(rk, koff, sbase + (j >> 1) * 16384 + (j & 1) * 128, AUX_NT);
  }
  PIN();
  __builtin_amdgcn_s_barrier();
  asm volatile("" ::: "memory");
  PIN();
  unsigned tag;
  unsigned long long tk2;
  asm volatile("s_waitcnt lgkmcnt(0)\n\ts_add_u32 %0, %2, 1\n\ts_mov_b64 %1, %3" : "=&s"(tag), "=&s"(tk2) : "s"(epoch), "s"(tk) : "memory", "scc");
  unsigned tz = (unsigned)tk2;
  const Tag T = mktag(tag);
  const rsrc_t rws = mkrsrc(ws, WS_BYTES);

  if (isV) {
    {
      float s = 0.f;
#pragma unroll
      for (int i = 0; i < 4; ++i)
#pragma unroll
        for (int k = 0; k < 4; ++k) s += lo16(R[i][k]) + hi16(R[i][k]);
      s += dpp<XOR1>(s); s += dpp<XOR2>(s);
      const float mean = xrow_sum(s) * (1.0f / 512.0f);
      float q = 0.f;
#pragma unroll
      for (int i = 0; i < 4; ++i)
#pragma unroll
        for (int k = 0; k < 4; ++k) {
          const float d0 = lo16(R[i][k]) - mean, d1 = hi16(R[i][k]) - mean;
          q = fmaf(d0, d0, q); q = fmaf(d1, d1, q);
        }
      q += dpp<XOR1>(q); q += dpp<XOR2>(q);
      const float var = rbf(xrow_sum(q) * (1.0f / 512.0f));
      const float den = rbf(__builtin_amdgcn_sqrtf(rbf(var + 1e-5f)));
      const float inv = __builtin_amdgcn_rcpf(den), mb = rbf(mean);
      v4u A;
#pragma unroll
      for (int k = 0; k < 4; ++k) {
        const unsigned d = pk2(lo16(R[0][k]) - mb, hi16(R[0][k]) - mb);
        const unsigned a = pk2(lo16(d) * inv, hi16(d) * inv), a2 = dppu<0xEE>(a);
        A[k] = (w4 & 1) ? a2 : a;
      }
      f32x4 acc[8];
      {
        v4u bf[8];
#pragma unroll
        for (int t = 0; t < 4; ++t) {
#pragma unroll
          for (int i = 0; i < 8; ++i) {
            bf[i][t] = permsel(R[4 + 2 * t], R[5 + 2 * t], i);
            if (t == 3) acc[i] = mfma(A, bf[i], f32x4{0.f, 0.f, 0.f, 0.f});
          }
          PIN();
        }
      }
      *(LAS f32x4*)(L + L_PX + w4 * 1024 + lane * 16) = fold8(acc, rs);
      LGKM0();
      unsigned old = 0u;
      if (lane == 0) old = __hip_atomic_fetch_add((LAS unsigned*)(flag + 1), 1u, __ATOMIC_RELAXED, __HIP_MEMORY_SCOPE_WORKGROUP);
      if (__builtin_amdgcn_readfirstlane(old) == 3u && has_tile) {
        asm volatile("" ::: "memory");
        const f32x4 o = (*(LAS f32x4*)(L + L_PX + lane * 16) + *(LAS f32x4*)(L + L_PX + 1024 + lane * 16)) +
                        (*(LAS f32x4*)(L + L_PX + 2048 + lane * 16) + *(LAS f32x4*)(L + L_PX + 3072 + lane * 16));
        st16(rws, WS_PG + tile * 1024 + lane * 16, unit_se(o, T));
      }
      PIN();
    }
    __builtin_amdgcn_s_setprio(0);
    if (w4 == 0) {
      pace(tz, CKT);
      const v2u ck = __builtin_amdgcn_raw_buffer_load_b64(rws, (lane & 7) * 128, WS_CK, AUX_SC1);
      const int dt = mean_offset(ck, tz, tag);
      if (lane == 0) flag[8] = (tz + dt + GOFF) | 1u;
    }
    lds_wait<4>(flag + 8, 1u);
    tz = __builtin_amdgcn_readfirstlane(flag[8]) - jit;
    const rsrc_t rv = mkrsrc(vc, 4u * 8 * 4096 * 128 * 2);
    const int voff = lane * 16;
    v4u V[32];
    v4u bf[4][8];
#define VPERM(pr) do { _Pragma("unroll") for (int i = 0; i < 8; ++i) bf[(pr) >> 2][i][(pr) & 3] = permsel(V[2 * (pr)], V[2 * (pr) + 1], i); } while (0)
#pragma unroll
    for (int j = 0; j < 32; ++j) {
      if (j >= VD) VMCNT(VD - 1);
      if ((j & 1) == 0) pace(tz, pt(32 + j));
      V[j] = __builtin_amdgcn_raw_buffer_load_b128(rv, voff, sbase + ((j & 24) + 4 * (j & 1) + ((j >> 1) & 3)) * 8192, AUX_NT);
      PIN();
      if (j >= VD + 1 && ((j - VD) & 1)) { VPERM((j - VD) >> 1); PIN(); }
    }
    lds_wait<1>(flag + 3, 4u);
    v4u P[4];
#pragma unroll
    for (int G = 0; G < 4; ++G) P[G] = *(LAS v4u*)(L + L_P + w4 * 1024 + G * 256 + (lane & ~3) * 4);
    f32x4 acc[8];
#pragma unroll
    for (int i = 0; i < 8; ++i) acc[i] = f32x4{0.f, 0.f, 0.f, 0.f};
    constexpr int DONE = (32 - VD) >> 1;
#pragma unroll
    for (int G = 0; G < 4; ++G) {
#pragma unroll
      for (int t = 0; t < 4; ++t)
        if (4 * G + t >= DONE) { VPERM(4 * G + t); if (t < 3) PIN(); }
#pragma unroll
      for (int i = 0; i < 8; ++i) acc[i] = mfma(P[G], bf[G][i], acc[i]);
      PIN();
    }
    *(LAS f32x4*)(L + L_ORED + w4 * 2048 + lane * 16) = f32x4{acc[0][0], acc[1][0], acc[2][0], acc[3][0]};
    *(LAS f32x4*)(L + L_ORED + w4 * 2048 + 1024 + lane * 16) = f32x4{acc[4][0], acc[5][0], acc[6][0], acc[7][0]};
    lds_inc(flag + 5, lane);
    lds_wait<0>(flag + 5, 4u);
    if (lane < 32) {
      const int ro = (lane & 1) * 1024 + (w4 * 16 + (lane >> 1)) * 16;
      const f32x4 o = (*(LAS f32x4*)(L + L_ORED + ro) + *(LAS f32x4*)(L + L_ORED + 2048 + ro)) +
                      (*(LAS f32x4*)(L + L_ORED + 4096 + ro) + *(LAS f32x4*)(L + L_ORED + 6144 + ro));
      st16(rws, WS_OG + ((b * 8 + xcd) * 4 + w4) * 4096 + g * 2048 + split * 256 + c * 16, unit(o, T));
    }
    PIN();
    lds_inc(flag + 6, lane);
    asm volatile("" ::"v"(pf));
    return;
  }

  v4u K[32];
#pragma unroll
  for (int j = 0; j < KPRE; ++j) K[j] = R[j];
  if (w4 == 0 && idx == 0 && lane == 0) __builtin_amdgcn_raw_buffer_store_b64(v2u{tz, tag}, rws, WS_CK + xcd * 128, 0, AUX_SC1);
  tz -= jit;
  PIN();
#pragma unroll
  for (int j = KPRE; j < 32; ++j) {
    if ((j & 1) == 0) pace(tz, pt(j));
    K[j] = __builtin_amdgcn_raw_buffer_load_b128(rk, koff, sbase + (j >> 1) * 16384 + (j & 1) * 128, AUX_NT);
    PIN();
  }
  {
    {
      const int off[1] = {WS_PG + (xcd * 16 + 4 * w4 + g) * 1024 + b * 256 + c * 16};
      v4u gv[1] = {ld16(rws, off[0])};
      asm volatile("" ::"v"(pf));
      settle<1, 8>(rws, off, gv, T);
      const f32x4 qv = uval_se(gv[0]);
      const float q0 = fold16(qv[0], qv[2]), q1 = fold16(qv[1], qv[3]);
      *(LAS unsigned*)(L + L_QS + (128 * w4 + 64 * (g >> 1) + 8 * (c & 7) + 4 * (c >> 3) + 2 * (g & 1)) * 2) = pk2(q0, q1);
      lds_inc(flag, lane);
      lds_wait<1>(flag, 4u);
    }
    v4u qf[2][2];
    {
      const bool a1 = (c & 2) != 0;
#pragma unroll
      for (int p = 0; p < 2; ++p) {
        const v4u t = *(LAS v4u*)(L + L_QS + (c >> 2) * 256 + (8 * p + 2 * g + (c & 1)) * 16);
#pragma unroll
        for (int k = 0; k < 4; ++k) { qf[p][0][k] = a1 ? 0u : t[k]; qf[p][1][k] = a1 ? t[k] : 0u; }
      }
    }
    float s[8];
    {
      f32x4 d[8];
#pragma unroll
      for (int n = 0; n < 8; ++n) d[n] = f32x4{0.f, 0.f, 0.f, 0.f};
#pragma unroll
      for (int z = 0; z < 4; ++z) {
#pragma unroll
        for (int n = 0; n < 8; ++n) d[n] = mfma(K[4 * n + z], qf[z & 1][z >> 1], d[n]);
        PIN();
      }
      const bool u1 = (c & 1) != 0;
#pragma unroll
      for (int n = 0; n < 8; ++n) {
        const float d0 = opq(d[n][0]), d1 = opq(d[n][1]), d2 = opq(d[n][2]), d3 = opq(d[n][3]);
        s[n] = (u1 ? d3 : d0) + dpp<XOR1>(u1 ? d1 : d2);
      }
    }
    float m = -INFINITY;
#pragma unroll
    for (int n = 0; n < 8; n += 2) {
      const unsigned r1 = pk2(s[n], s[n + 1]);
      const unsigned r2 = pk2(lo16(r1) * SCALE, hi16(r1) * SCALE);
      s[n] = lo16(r2); s[n + 1] = hi16(r2);
      m = fmaxf(m, fmaxf(s[n], s[n + 1]));
    }
    m = fmaxf(m, dpp<XOR1>(m)); m = fmaxf(m, dpp<XOR2>(m));
    m = xrow_max(m);
    if (g == 0 && (c & 3) == 0) *(LAS float*)(L + L_MX + ((c >> 2) * 4 + w4) * 4) = m;
    lds_inc(flag + 2, lane);
    lds_wait<1>(flag + 2, 4u);
    {
      const f32x4 ma = *(LAS f32x4*)(L + L_MX + (c >> 2) * 16);
      const float M = fmaxf(fmaxf(ma[0], ma[1]), fmaxf(ma[2], ma[3]));
      float l = 0.f;
#pragma unroll
      for (int G = 0; G < 4; ++G) {
        const float p0 = __builtin_amdgcn_exp2f((s[2 * G] - M) * LOG2E), p1 = __builtin_amdgcn_exp2f((s[2 * G + 1] - M) * LOG2E);
        l += p0 + p1;
        *(LAS unsigned*)(L + L_P + w4 * 1024 + G * 256 + lane * 4) = pk2(p0, p1);
      }
      l += dpp<XOR1>(l); l += dpp<XOR2>(l);
      l = xrow_sum(l);
      if (g == 0 && (c & 3) == 0) *(LAS float*)(L + L_LS + ((c >> 2) * 4 + w4) * 4) = l;
      lds_inc(flag + 3, lane);
    }
  }
  const int kvc = (2 * (xcd & 3) + 2 + 4 * (xcd >> 2) + (idx >> 4)) & 7, r8 = (idx >> 1) & 7, cg = 2 * (xcd >> 2) + (idx & 1);
  const int cgf = 2 * (1 - (xcd >> 2)) + (xcd & 1), bf = 2 * ((xcd >> 1) & 1) + (idx >> 4), fa = idx & 15;
  const int rg = kvc * 8 + r8;
  const int fc = 2 * (fa & 7) + (lane & 1);
  const int col = 128 * cgf + 64 * (fa >> 3) + 8 * (fc & 7) + 4 * (fc >> 3) + 2 * (g >> 1) + (g & 1);
  v4u W[12];
  unsigned xr = 0u;
  if (w4 == 0) {
    xr = __builtin_amdgcn_raw_buffer_load_b16(mkrsrc(x, 4096u), (bf * 512 + col) * 2, 0, 0);
    lds_wait<1>(flag + 3, 4u);
    if (lane < 4) {
      const f32x4 ma = *(LAS f32x4*)(L + L_MX + lane * 16), la = *(LAS f32x4*)(L + L_LS + lane * 16);
      st16(rws, WS_ML + ((b * 8 + xcd) * 4 + lane) * 128 + split * 16,
           unit(f32x4{fmaxf(fmaxf(ma[0], ma[1]), fmaxf(ma[2], ma[3])), (la[0] + la[1]) + (la[2] + la[3]), 0.f, 0.f}, T));
    }
  } else if (w4 == 1) {
    const int qd = c & 7, f = 28 * rg + 4 * (qd < 7 ? qd : 6);
    const int vo = WS_PG + (128 + 2 * (f >> 6) + (c >> 3)) * 1024 + g * 256 + (((f >> 2) & 1) * 8 + ((f & 63) >> 3)) * 16;
    const int off[2] = {vo, vo + 56 * 1024};
    v4u gv[2] = {ld16(rws, off[0]), ld16(rws, off[1])};
    settle<2, 16>(rws, off, gv, T);
    const f32x4 gp = uval_se(gv[0]), upp = uval_se(gv[1]);
    float hv[4];
#pragma unroll
    for (int k = 0; k < 4; ++k) {
      const float g0 = gp[k], u0 = upp[k];
      const float gt = rbf(g0 + dpp<ROR8>(g0)), up = rbf(u0 + dpp<ROR8>(u0));
      const float sl = rbf(gt * __builtin_amdgcn_rcpf(1.0f + __builtin_amdgcn_exp2f(-gt * LOG2E)));
      hv[k] = sl * up;
    }
    if (c < 8) *(LAS v2u*)(L + L_HA + g * 64 + c * 8) = qd < 7 ? v2u{pk2(hv[0], hv[1]), pk2(hv[2], hv[3])} : v2u{0u, 0u};
  }
  float w0, w1_;
  {
    lds_wait<4>(flag + 8, 1u);
    tz = __builtin_amdgcn_readfirstlane(flag[8]) - jit;
    pace(tz, PEND - MLT);
    const int mloff[1] = {WS_ML + ((w4 * 8 + kvc) * 4 + (r8 >> 1)) * 128 + (4 * (c & 1) + g) * 16};
    v4u ml[1] = {ld16(rws, mloff[0])};
    settle<1, 4>(rws, mloff, ml, T);
    const float ms = uval(ml[0][0]);
    const float M = xrow_max(fmaxf(ms, dpp<XOR1>(ms)));
    float wa = __builtin_amdgcn_exp2f((ms - M) * LOG2E);
    float Ls = wa * uval(ml[0][1]);
    Ls = xrow_sum(Ls + dpp<XOR1>(Ls));
    wa *= __builtin_amdgcn_rcpf(Ls);
    w0 = dpp<0xA0>(wa); w1_ = dpp<0xF5>(wa);
    asm volatile("" : "+v"(w0), "+v"(w1_));
  }
  if (w4 >= 2) {
    __builtin_amdgcn_s_setprio(0);
    const rsrc_t rwo = mkrsrc(wo, 4096u * 512 * 2), rw2 = mkrsrc(w2, 1792u * 512 * 2);
    const int colb = (128 * cg + 64 * (w4 - 2)) * 2;
    const int vo = ((c >> 3) * 32 + g * 8) * 1024 + (c & 7) * 16;
    const int vo2 = (rs && g == 3) ? 0x7ff00000 : ((c >> 3) * 16 + g * 4) * 1024 + (c & 7) * 16;
    asm volatile("" ::"v"(vo), "v"(vo2));
#pragma unroll
    for (int n = 0; n < 12; ++n) {
      if (n % 4 == 0) pace(tz, PEND + WOT + (n * PD) / 20);
      if (n < 4) W[8 + n] = __builtin_amdgcn_raw_buffer_load_b128(rw2, vo2, (28 * rg + n) * 1024 + colb, AUX_NT);
      else W[n - 4] = __builtin_amdgcn_raw_buffer_load_b128(rwo, vo, (64 * rg + n - 4) * 1024 + colb, AUX_NT);
      PIN();
    }
    PIN();
  }
  v4u Bo[8];
  f32x4 acc[8];
  lds_inc(flag + 7, lane);
  lds_wait<1>(flag + 6, 4u);
  {
    __builtin_amdgcn_s_sleep(PDELAY1);
    const int so = WS_OG + ((w4 * 8 + kvc) * 8 + r8) * 2048 + lane * 16;
    const int off[2] = {so, so + 1024};
    v4u gv[2] = {ld16(rws, off[0]), ld16(rws, off[1])};
    PIN();
    if (w4 >= 2) {
      lds_wait<1>(flag + 7, 4u);
      const v2u ha = *(LAS v2u*)(L + L_HA + (c >> 2) * 64 + (c & 1) * 32 + g * 8);
#pragma unroll
      for (int i = 0; i < 8; ++i)
        acc[i] = mfma(v4u{ha[0], ha[1], 0u, 0u}, v4u{permsel(W[8], W[9], i), permsel(W[10], W[11], i), 0u, 0u}, f32x4{0.f, 0.f, 0.f, 0.f});
      PIN();
#pragma unroll
      for (int t = 0; t < 4; ++t) {
#pragma unroll
        for (int i = 0; i < 8; ++i) Bo[i][t] = permsel(W[2 * t], W[2 * t + 1], i);
        PIN();
      }
    }
    PIN();
    settle<2, 1>(rws, off, gv, T);
    float o[4];
#pragma unroll
    for (int k = 0; k < 4; ++k) o[k] = w0 * uval(gv[0][k]) + w1_ * uval(gv[1][k]);
    const float r = fold16(fold32(o[0], o[2]), fold32(o[1], o[3]));
    *(LAS u16*)(L + L_OA + w4 * 128 + (4 * c + g) * 2) = (u16)pk2(r, r);
  }
  wg_barrier();
  if (w4 >= 2) {
    const v4u a0 = *(LAS v4u*)(L + L_OA + (c >> 2) * 128 + (c & 1) * 64 + g * 16);
#pragma unroll
    for (int i = 0; i < 8; ++i) acc[i] = mfma(a0, Bo[i], acc[i]);
    st16(rws, WS_YG + ((cg * 64 + rg) * 2 + (w4 - 2)) * 1024 + lane * 16, unit(fold8(acc, rs), T));
    wg_barrier();
  } else if (w4 == 0) {
    wg_barrier();
    __builtin_amdgcn_s_sleep(PDELAY2);
    const int vy = WS_YG + (cgf * 64 + (lane >> 1)) * 2048 + (fa >> 3) * 1024 + bf * 256 + fc * 16;
    const int off[2] = {vy, vy + 32 * 2048};
    v4u y[2] = {ld16(rws, off[0]), ld16(rws, off[1])};
    settle<2, 1>(rws, off, y, T);
    float o[4];
#pragma unroll
    for (int k = 0; k < 4; ++k) o[k] = uval(y[0][k]) + uval(y[1][k]);
    float r = fold16(fold32(o[0], o[2]), fold32(o[1], o[3]));
    r += dpp<XOR2>(r); r += dpp<ROR4>(r); r += dpp<ROR8>(r);
    r += lo16(xr);
    if (c < 2) __builtin_amdgcn_raw_buffer_store_b16((u16)pk2(r, r), mkrsrc(out, 4096u), (bf * 512 + col) * 2, 0, AUX_SC1);
    if (bid == 0 && lane == 0) __builtin_amdgcn_raw_buffer_store_b32(tag, rws, 0, WS_E, AUX_SC1);
  }
}

extern "C" void kernel_launch(void* const* d_in, const int* in_sizes, int n_in, void* d_out, int out_size, void* d_ws, size_t ws_size,
                              hipStream_t stream) {
  (void)in_sizes; (void)n_in; (void)out_size; (void)ws_size;
  fused_decode<<<256, NTHR, 0, stream>>>((const u16*)d_in[0], (const u16*)d_in[1], (const u16*)d_in[2], (const u16*)d_in[3], (const u16*)d_in[4],
                                         (const u16*)d_in[6], (const u16*)d_in[7], (const u16*)d_in[8], (u16*)d_out, (unsigned char*)d_ws);
}
```
